```python
import torch

def _softmax(x, axis=-1):
    return torch.softmax(x, dim=axis)
def _gelu(x):
    return torch.nn.functional.gelu(x, approximate="tanh")
def _silu(x):
    return torch.nn.functional.silu(x)
def _sigmoid(x):
    return torch.sigmoid(x)
def _layernorm(x, axis=-1, eps=1e-5):
    m = x.mean(dim=axis, keepdim=True)
    v = x.var(dim=axis, keepdim=True, unbiased=False)
    return (x - m) / torch.sqrt(v + eps)
def _scatter_add(tgt, idx, src, axis=0):
    return tgt.clone().index_add_(axis, idx, src)
def _segment_sum(src, seg, n):
    out = torch.zeros((n,) + src.shape[1:], dtype=src.dtype)
    return out.index_add_(0, seg, src)
def _pad(x, pw):
    flat = tuple(v for pair in reversed(pw) for v in pair)
    return torch.nn.functional.pad(x, flat)

kernel_name = 'sequential_preLN__spectral_fnet__dense_activation_squared_relu__prompt'

def setup_inputs():

    _w = (lambda a: (lambda t: t.to(torch.bfloat16) if t.is_floating_point() else t)(torch.as_tensor(a)))
    def _A_setup():
        B, S, D = 1, 2048, 640
        ns = torch.arange(S)[:, None] * torch.arange(S)[None, :]
        nd = torch.arange(D)[:, None] * torch.arange(D)[None, :]
        Ws_re = torch.cos(-2.0 * torch.pi * ns / S).to(float)
        Ws_im = torch.sin(-2.0 * torch.pi * ns / S).to(float)
        Wd_re = torch.cos(-2.0 * torch.pi * nd / D).to(float)
        Wd_im = torch.sin(-2.0 * torch.pi * nd / D).to(float)
        return {
            "x": _w((lambda *s: torch.randn(*s, dtype=torch.bfloat16) * 0.05)(B, S, D)),
            "dft_seq_re": _w(Ws_re),
            "dft_seq_im": _w(Ws_im),
            "dft_hid_re": _w(Wd_re),
            "dft_hid_im": _w(Wd_im),
        }
    _A_in = _A_setup()

    def _B_setup():
        B, S, D, F = 1, 2048, 640, 1536
        return {
            "x": _w((lambda *s: torch.randn(*s, dtype=torch.bfloat16) * 0.05)(B, S, D)),
            "w1": _w((lambda *s: torch.randn(*s, dtype=torch.bfloat16) * 0.2)(D, F)),
            "b1": _w((lambda *s: torch.randn(*s, dtype=torch.bfloat16) * 0.2)(F)),
            "w2": _w((lambda *s: torch.randn(*s, dtype=torch.bfloat16) * 0.2)(F, D)),
            "b2": _w((lambda *s: torch.randn(*s, dtype=torch.bfloat16) * 0.2)(D)),
        }
    _B_in = _B_setup()

    return {
        "A_x": _A_in["x"],
        "A_dft_seq_re": _A_in["dft_seq_re"],
        "A_dft_seq_im": _A_in["dft_seq_im"],
        "A_dft_hid_re": _A_in["dft_hid_re"],
        "A_dft_hid_im": _A_in["dft_hid_im"],
        "B_x": _B_in["x"],
        "B_w1": _B_in["w1"],
        "B_b1": _B_in["b1"],
        "B_w2": _B_in["w2"],
        "B_b2": _B_in["b2"],
    }

def reference(A_x, A_dft_seq_re, A_dft_seq_im, A_dft_hid_re, A_dft_hid_im, B_x, B_w1, B_b1, B_w2, B_b2):
    def _A_ref(x, dft_seq_re, dft_seq_im, dft_hid_re, dft_hid_im):
        a = torch.einsum("bsd,ed->bse", x, dft_hid_re)
        b = torch.einsum("bsd,ed->bse", x, dft_hid_im)
        y = torch.einsum("ts,bse->bte", dft_seq_re, a) - torch.einsum("ts,bse->bte", dft_seq_im, b)
        return y

    def _B_ref(x, w1, b1, w2, b2):
        h = torch.einsum("bsd,df->bsf", x, w1) + b1
        zero = torch.zeros_like(h)
        r = torch.where(h > zero, h, zero)
        h = r * r
        out = torch.einsum("bsf,fd->bsd", h, w2) + b2
        return out

    x = A_x
    h = x + _A_ref(_layernorm(x, axis=-1), A_dft_seq_re, A_dft_seq_im, A_dft_hid_re, A_dft_hid_im)
    h2 = h + _B_ref(_layernorm(h, axis=-1), B_w1, B_b1, B_w2, B_b2)
    return h2
```

```cpp
#include <hip/hip_runtime.h>
#include <hip/hip_bf16.h>
#include <stdint.h>

typedef short bf16x8 __attribute__((ext_vector_type(8)));
typedef float f32x4 __attribute__((ext_vector_type(4)));
typedef unsigned short u16;

__device__ __forceinline__ float bf2f(u16 v) { return __uint_as_float(((unsigned)v) << 16); }
__device__ __forceinline__ u16 f2bf(float f) { __hip_bfloat16 b = __float2bfloat16(f); return *(u16*)&b; }

constexpr int S = 2048, D = 640, F = 1536;

__global__ void ln_kernel(const u16* __restrict__ x, u16* __restrict__ xn) {
  int row = blockIdx.x; int lane = threadIdx.x;
  float v[10]; float s = 0;
  for (int i = 0; i < 10; ++i) { v[i] = bf2f(x[row * D + lane + 64 * i]); s += v[i]; }
  for (int o = 32; o; o >>= 1) s += __shfl_xor(s, o);
  float m = s / D; float q = 0;
  for (int i = 0; i < 10; ++i) { float d = v[i] - m; q += d * d; }
  for (int o = 32; o; o >>= 1) q += __shfl_xor(q, o);
  float r = rsqrtf(q / D + 1e-5f);
  for (int i = 0; i < 10; ++i) xn[row * D + lane + 64 * i] = f2bf((v[i] - m) * r);
}

__global__ void transpose_kernel(const u16* __restrict__ in, u16* __restrict__ out, int R, int C) {
  int i = blockIdx.x * blockDim.x + threadIdx.x;
  if (i < R * C) { int r = i / C, c = i % C; out[c * R + r] = in[i]; }
}

template <int MODE>
__global__ void gemm_kernel(const u16* __restrict__ A, const u16* __restrict__ B, const u16* __restrict__ A2, const u16* __restrict__ B2,
                            int K, int lda, int ldb, u16* __restrict__ C, int ldc, const u16* __restrict__ bias, const u16* __restrict__ res) {
  int lane = threadIdx.x & 63;
  int m0 = blockIdx.y * 32, n0 = blockIdx.x * 32;
  int fr = lane & 15, fq = lane >> 4;
  f32x4 acc[2][2] = {};
  f32x4 acc2[2][2] = {};
  for (int k = 0; k < K; k += 32) {
    bf16x8 a[2], b[2];
    for (int i = 0; i < 2; ++i) {
      a[i] = *(const bf16x8*)(A + (size_t)(m0 + i * 16 + fr) * lda + k + fq * 8);
      b[i] = *(const bf16x8*)(B + (size_t)(n0 + i * 16 + fr) * ldb + k + fq * 8);
    }
    for (int i = 0; i < 2; ++i) for (int j = 0; j < 2; ++j)
      acc[i][j] = __builtin_amdgcn_mfma_f32_16x16x32_bf16(a[i], b[j], acc[i][j], 0, 0, 0);
    if (MODE == 1) {
      for (int i = 0; i < 2; ++i) {
        a[i] = *(const bf16x8*)(A2 + (size_t)(m0 + i * 16 + fr) * lda + k + fq * 8);
        b[i] = *(const bf16x8*)(B2 + (size_t)(n0 + i * 16 + fr) * ldb + k + fq * 8);
      }
      for (int i = 0; i < 2; ++i) for (int j = 0; j < 2; ++j)
        acc2[i][j] = __builtin_amdgcn_mfma_f32_16x16x32_bf16(a[i], b[j], acc2[i][j], 0, 0, 0);
    }
  }
  for (int i = 0; i < 2; ++i) for (int j = 0; j < 2; ++j) for (int r = 0; r < 4; ++r) {
    int m = m0 + i * 16 + fq * 4 + r, n = n0 + j * 16 + fr;
    float v = acc[i][j][r];
    if (MODE == 1) v = v - acc2[i][j][r] + bf2f(res[(size_t)m * ldc + n]);
    if (MODE == 2) { v += bf2f(bias[n]); v = v > 0.f ? v : 0.f; v = v * v; }
    if (MODE == 3) v = v + bf2f(bias[n]) + bf2f(res[(size_t)m * ldc + n]);
    C[(size_t)m * ldc + n] = f2bf(v);
  }
}

extern "C" void kernel_launch(void* const* d_in, const int* in_sizes, int n_in,
                              void* d_out, int out_size, void* d_ws, size_t ws_size,
                              hipStream_t stream) {
  const u16* x = (const u16*)d_in[0];
  const u16* Cr = (const u16*)d_in[1];
  const u16* Ci = (const u16*)d_in[2];
  const u16* Dr = (const u16*)d_in[3];
  const u16* Di = (const u16*)d_in[4];
  const u16* w1 = (const u16*)d_in[6];
  const u16* b1 = (const u16*)d_in[7];
  const u16* w2 = (const u16*)d_in[8];
  const u16* b2 = (const u16*)d_in[9];
  u16* out = (u16*)d_out;
  u16* ws = (u16*)d_ws;
  u16* xn = ws; ws += S * D;
  u16* aT = ws; ws += D * S;
  u16* bT = ws; ws += D * S;
  u16* h = ws; ws += S * D;
  u16* hn = ws; ws += S * D;
  u16* H = ws; ws += S * F;
  u16* w1T = ws; ws += D * F;
  u16* w2T = ws; ws += D * F;
  ln_kernel<<<S, 64, 0, stream>>>(x, xn);
  transpose_kernel<<<(D * F + 255) / 256, 256, 0, stream>>>(w1, w1T, D, F);
  transpose_kernel<<<(D * F + 255) / 256, 256, 0, stream>>>(w2, w2T, F, D);
  gemm_kernel<0><<<dim3(S / 32, D / 32), 64, 0, stream>>>(Dr, xn, nullptr, nullptr, D, D, D, aT, S, nullptr, nullptr);
  gemm_kernel<0><<<dim3(S / 32, D / 32), 64, 0, stream>>>(Di, xn, nullptr, nullptr, D, D, D, bT, S, nullptr, nullptr);
  gemm_kernel<1><<<dim3(D / 32, S / 32), 64, 0, stream>>>(Cr, aT, Ci, bT, S, S, S, h, D, nullptr, x);
  ln_kernel<<<S, 64, 0, stream>>>(h, hn);
  gemm_kernel<2><<<dim3(F / 32, S / 32), 64, 0, stream>>>(hn, w1T, nullptr, nullptr, D, D, D, H, F, b1, nullptr);
  gemm_kernel<3><<<dim3(D / 32, S / 32), 64, 0, stream>>>(H, w2T, nullptr, nullptr, F, F, F, out, D, b2, h);
}
```

```cpp
#include <hip/hip_runtime.h>
#include <hip/hip_bf16.h>
#include <stdint.h>

typedef short bf16x8 __attribute__((ext_vector_type(8)));
typedef float f32x4 __attribute__((ext_vector_type(4)));
typedef unsigned short u16;

__device__ __forceinline__ float bf2f(u16 v) { return __uint_as_float(((unsigned)v) << 16); }
__device__ __forceinline__ u16 f2bf(float f) { __hip_bfloat16 b = __float2bfloat16(f); return *(u16*)&b; }

constexpr int S = 2048, D = 640, F = 1536;

__global__ void ln_kernel(const u16* __restrict__ x, u16* __restrict__ xn) {
  int row = blockIdx.x; int lane = threadIdx.x;
  float v[10]; float s = 0;
  for (int i = 0; i < 10; ++i) { v[i] = bf2f(x[row * D + lane + 64 * i]); s += v[i]; }
  for (int o = 32; o; o >>= 1) s += __shfl_xor(s, o);
  float m = s / D; float q = 0;
  for (int i = 0; i < 10; ++i) { float d = v[i] - m; q += d * d; }
  for (int o = 32; o; o >>= 1) q += __shfl_xor(q, o);
  float r = rsqrtf(q / D + 1e-5f);
  for (int i = 0; i < 10; ++i) xn[row * D + lane + 64 * i] = f2bf((v[i] - m) * r);
}

__global__ void transpose_kernel(const u16* __restrict__ in, u16* __restrict__ out, int R, int C) {
  int i = blockIdx.x * blockDim.x + threadIdx.x;
  if (i < R * C) { int r = i / C, c = i % C; out[c * R + r] = in[i]; }
}

template <int MODE>
__global__ void gemm_kernel(const u16* __restrict__ A, const u16* __restrict__ B, const u16* __restrict__ A2, const u16* __restrict__ B2,
                            int K, int lda, int ldb, u16* __restrict__ C, int ldc, const u16* __restrict__ bias, const u16* __restrict__ res) {
  int lane = threadIdx.x & 63;
  int m0 = blockIdx.y * 32, n0 = blockIdx.x * 32;
  int fr = lane & 15, fq = lane >> 4;
  f32x4 acc[2][2] = {};
  f32x4 acc2[2][2] = {};
  for (int k = 0; k < K; k += 32) {
    bf16x8 a[2], b[2];
    for (int i = 0; i < 2; ++i) {
      a[i] = *(const bf16x8*)(A + (size_t)(m0 + i * 16 + fr) * lda + k + fq * 8);
      b[i] = *(const bf16x8*)(B + (size_t)(n0 + i * 16 + fr) * ldb + k + fq * 8);
    }
    for (int i = 0; i < 2; ++i) for (int j = 0; j < 2; ++j)
      acc[i][j] = __builtin_amdgcn_mfma_f32_16x16x32_bf16(a[i], b[j], acc[i][j], 0, 0, 0);
    if (MODE == 1) {
      for (int i = 0; i < 2; ++i) {
        a[i] = *(const bf16x8*)(A2 + (size_t)(m0 + i * 16 + fr) * lda + k + fq * 8);
        b[i] = *(const bf16x8*)(B2 + (size_t)(n0 + i * 16 + fr) * ldb + k + fq * 8);
      }
      for (int i = 0; i < 2; ++i) for (int j = 0; j < 2; ++j)
        acc2[i][j] = __builtin_amdgcn_mfma_f32_16x16x32_bf16(a[i], b[j], acc2[i][j], 0, 0, 0);
    }
  }
  for (int i = 0; i < 2; ++i) for (int j = 0; j < 2; ++j) for (int r = 0; r < 4; ++r) {
    int m = m0 + i * 16 + fq * 4 + r, n = n0 + j * 16 + fr;
    float v = acc[i][j][r];
    if (MODE == 1) v = v - acc2[i][j][r] + bf2f(res[(size_t)m * ldc + n]);
    if (MODE == 2) { v += bf2f(bias[n]); v = v > 0.f ? v : 0.f; v = v * v; }
    if (MODE == 3) v = v + bf2f(bias[n]) + bf2f(res[(size_t)m * ldc + n]);
    C[(size_t)m * ldc + n] = f2bf(v);
  }
}


__global__ void chase_kernel(const unsigned* __restrict__ p, int n, int stride_words, unsigned* sink) {
  unsigned off = 0, acc = 0;
  for (int i = 0; i < n; ++i) {
    unsigned v;
    const unsigned* q = p + off;
    asm volatile("global_load_dword %0, %1, off\n\ts_waitcnt vmcnt(0)" : "=v"(v) : "v"(q) : "memory");
    acc ^= v;
    off += stride_words + (v & 0);
    asm volatile("" : "+v"(off));
  }
  if (acc == 0x12345678) sink[0] = acc;
}

extern "C" void kernel_launch(void* const* d_in, const int* in_sizes, int n_in,
                              void* d_out, int out_size, void* d_ws, size_t ws_size,
                              hipStream_t stream) {
  const u16* x = (const u16*)d_in[0];
  const u16* Cr = (const u16*)d_in[1];
  const u16* Ci = (const u16*)d_in[2];
  const u16* Dr = (const u16*)d_in[3];
  const u16* Di = (const u16*)d_in[4];
  const u16* w1 = (const u16*)d_in[6];
  const u16* b1 = (const u16*)d_in[7];
  const u16* w2 = (const u16*)d_in[8];
  const u16* b2 = (const u16*)d_in[9];
  u16* out = (u16*)d_out;
  u16* ws = (u16*)d_ws;
  u16* xn = ws; ws += S * D;
  u16* aT = ws; ws += D * S;
  u16* bT = ws; ws += D * S;
  u16* h = ws; ws += S * D;
  u16* hn = ws; ws += S * D;
  u16* H = ws; ws += S * F;
  u16* w1T = ws; ws += D * F;
  u16* w2T = ws; ws += D * F;
  ln_kernel<<<S, 64, 0, stream>>>(x, xn);
  transpose_kernel<<<(D * F + 255) / 256, 256, 0, stream>>>(w1, w1T, D, F);
  transpose_kernel<<<(D * F + 255) / 256, 256, 0, stream>>>(w2, w2T, F, D);
  gemm_kernel<0><<<dim3(S / 32, D / 32), 64, 0, stream>>>(Dr, xn, nullptr, nullptr, D, D, D, aT, S, nullptr, nullptr);
  gemm_kernel<0><<<dim3(S / 32, D / 32), 64, 0, stream>>>(Di, xn, nullptr, nullptr, D, D, D, bT, S, nullptr, nullptr);
  gemm_kernel<1><<<dim3(D / 32, S / 32), 64, 0, stream>>>(Cr, aT, Ci, bT, S, S, S, h, D, nullptr, x);
  ln_kernel<<<S, 64, 0, stream>>>(h, hn);
  gemm_kernel<2><<<dim3(F / 32, S / 32), 64, 0, stream>>>(hn, w1T, nullptr, nullptr, D, D, D, H, F, b1, nullptr);
  gemm_kernel<3><<<dim3(D / 32, S / 32), 64, 0, stream>>>(H, w2T, nullptr, nullptr, F, F, F, out, D, b2, h);
}
```

```cpp
#include <hip/hip_runtime.h>
#include <stdint.h>

struct Params {
  const void* x; const void* cr; const void* ci; const void* dr; const void* di;
  const void* w1; const void* b1; const void* w2; const void* b2;
  void* out; void* ws;
};

__global__ __launch_bounds__(256, 1) void fnet_fused(Params p) {
  __shared__ __attribute__((aligned(16))) char smem[163840];
  const unsigned w = __builtin_amdgcn_readfirstlane(threadIdx.x >> 6);
  const unsigned lds0 = (unsigned)(size_t)(__attribute__((address_space(3))) char*)smem;
  asm volatile(
R"A(s_load_dwordx16 s[48:63], %0, 0x0
s_load_dwordx4 s[64:67], %0, 0x40
s_load_dwordx2 s[68:69], %0, 0x50
s_mov_b32 s17, %1
s_mov_b32 s16, %2
v_mbcnt_lo_u32_b32 v0, -1, 0
v_mbcnt_hi_u32_b32 v0, -1, v0
s_lshr_b32 s77, s17, 3
v_lshrrev_b32_e32 v3, 4, v0
s_getpc_b64 s[12:13]
Lpc_%=:
s_lshl_b32 s90, s77, 2
s_add_u32 s90, s90, s16
s_lshl_b32 s90, s90, 9
v_lshl_add_u32 v253, v3, 7, s90
v_min_u32_e32 v253, Lend_%=-Lpc_%=-4, v253
global_load_dword v252, v253, s[12:13]
s_bfe_u32 s18, s17, 0x10002
s_lshr_b32 s19, s17, 6
s_and_b32 s73, s77, 7
s_and_b32 s90, s17, 3
s_lshl_b32 s72, s90, 2
s_add_u32 s72, s72, s19
s_lshr_b32 s74, s16, 1
s_and_b32 s75, s16, 1
v_lshrrev_b32_e32 v4, 3, v0
v_and_b32_e32 v5, 7, v0
v_xor_b32_e32 v5, v5, v4
v_lshlrev_b32_e32 v5, 4, v5
v_lshrrev_b32_e32 v6, 5, v0
v_and_b32_e32 v7, 3, v4
v_lshl_add_u32 v6, v6, 3, v7
s_movk_i32 s91, 1280
v_mad_u32_u24 v6, v6, s91, v5
s_lshl_b32 s90, s72, 2
s_add_u32 s90, s90, s16
s_mul_i32 s90, s90, 40960
v_add_u32_e32 v16, s90, v6
v_add_u32_e32 v17, 19456, v16
v_add_u32_e32 v18, 3072, v16
v_add_u32_e32 v19, 22528, v16
s_lshl_b32 s90, s73, 1
s_add_u32 s90, s90, s74
s_lshl_b32 s90, s90, 3
s_add_u32 s90, s90, s75
s_mul_i32 s90, s90, 5120
v_add_u32_e32 v20, s90, v6
v_add_u32_e32 v21, 19456, v20
s_lshl_b32 s90, s73, 1
s_add_u32 s90, s90, s75
s_add_u32 s90, s90, 64
s_mul_i32 s90, s90, 10240
v_mad_u32_u24 v22, v4, s91, v5
v_add_u32_e32 v22, s90, v22
s_lshl_b32 s78, s73, 7
s_lshr_b32 s90, s73, 2
s_add_u32 s90, s90, 8
s_lshl_b32 s79, s90, 7
s_lshl_b32 s99, s16, 12
s_lshl_b32 s94, s16, 13
s_lshl_b32 s96, s16, 11
s_lshl_b32 s97, s16, 10
s_mov_b32 s26, 2621440
s_mov_b32 s27, 0x20000
s_mov_b32 s30, 819200
s_mov_b32 s31, 0x20000
s_waitcnt lgkmcnt(0)
s_mov_b32 s24, s48
s_and_b32 s25, s49, 0xffff
s_cmp_eq_u32 s18, 0
s_cselect_b32 s28, s54, s56
s_cselect_b32 s29, s55, s57
s_cselect_b32 s32, s50, s52
s_cselect_b32 s33, s51, s53
s_and_b32 s29, s29, 0xffff
s_add_u32 m0, s99, 65536
s_nop 0
buffer_load_dwordx4 v16, s[24:27], s78 offen offset:0 lds
buffer_load_dwordx4 v17, s[24:27], s78 offen offset:1024 lds
buffer_load_dwordx4 v18, s[24:27], s78 offen offset:2048 lds
buffer_load_dwordx4 v19, s[24:27], s78 offen offset:3072 lds
s_add_u32 m0, s96, 81920
s_nop 0
buffer_load_dwordx4 v20, s[28:31], s78 offen offset:0 lds
buffer_load_dwordx4 v21, s[28:31], s78 offen offset:1024 lds
s_add_u32 m0, s97, 32768
s_cmp_eq_u32 s74, 0
s_cselect_b32 s91, s78, s79
buffer_load_dwordx4 v22, s[28:31], s91 offen offset:0 lds
s_add_u32 m0, s96, 106496
s_nop 0
buffer_load_dwordx4 v20, s[28:31], s79 offen offset:0 lds
buffer_load_dwordx4 v21, s[28:31], s79 offen offset:1024 lds
s_mov_b32 s20, s68
s_and_b32 s21, s69, 0xffff
s_mov_b32 s22, 20840448
s_mov_b32 s23, 0x20000
v_lshlrev_b32_e32 v1, 4, v0
s_lshl_b32 s88, s18, 3
s_add_u32 s88, s88, s73
s_mul_i32 s95, s88, 327680
s_add_u32 s95, s95, 131072
s_lshl_b32 s53, s88, 7
s_add_u32 s53, s53, 4096
s_lshl_b32 s54, s72, 3
s_add_u32 s54, s54, s53
s_lshl_b32 s89, s72, 2
s_add_u32 s89, s89, s16
s_mul_i32 s89, s89, 5120
s_add_u32 s89, s89, s95
s_xor_b32 s90, s97, 0
s_add_u32 s48, s90, s89
s_xor_b32 s90, s97, 1024
s_add_u32 s49, s90, s89
s_xor_b32 s90, s97, 2048
s_add_u32 s50, s90, s89
s_xor_b32 s90, s97, 3072
s_add_u32 s51, s90, s89
s_add_u32 s52, s89, 4096
s_xor_b32 s93, s79, 128
s_mul_i32 s90, s17, 1
s_add_u32 s90, s90, s19
s_add_u32 s90, s90, s19
s_add_u32 s88, s90, 0
s_and_b32 s88, s88, 7
s_cmp_ge_u32 s88, s73
s_addc_u32 s89, s88, 0
s_lshl_b32 s89, s89, 7
s_cmp_eq_u32 s88, 7
s_cselect_b32 s80, s93, s89
s_add_u32 s88, s90, 1
s_and_b32 s88, s88, 7
s_cmp_ge_u32 s88, s73
s_addc_u32 s89, s88, 0
s_lshl_b32 s89, s89, 7
s_cmp_eq_u32 s88, 7
s_cselect_b32 s81, s93, s89
s_add_u32 s88, s90, 2
s_and_b32 s88, s88, 7
s_cmp_ge_u32 s88, s73
s_addc_u32 s89, s88, 0
s_lshl_b32 s89, s89, 7
s_cmp_eq_u32 s88, 7
s_cselect_b32 s82, s93, s89
s_add_u32 s88, s90, 3
s_and_b32 s88, s88, 7
s_cmp_ge_u32 s88, s73
s_addc_u32 s89, s88, 0
s_lshl_b32 s89, s89, 7
s_cmp_eq_u32 s88, 7
s_cselect_b32 s83, s93, s89
s_add_u32 s88, s90, 4
s_and_b32 s88, s88, 7
s_cmp_ge_u32 s88, s73
s_addc_u32 s89, s88, 0
s_lshl_b32 s89, s89, 7
s_cmp_eq_u32 s88, 7
s_cselect_b32 s84, s93, s89
s_add_u32 s88, s90, 5
s_and_b32 s88, s88, 7
s_cmp_ge_u32 s88, s73
s_addc_u32 s89, s88, 0
s_lshl_b32 s89, s89, 7
s_cmp_eq_u32 s88, 7
s_cselect_b32 s85, s93, s89
s_add_u32 s88, s90, 6
s_and_b32 s88, s88, 7
s_cmp_ge_u32 s88, s73
s_addc_u32 s89, s88, 0
s_lshl_b32 s89, s89, 7
s_cmp_eq_u32 s88, 7
s_cselect_b32 s86, s93, s89
s_add_u32 s88, s90, 7
s_and_b32 s88, s88, 7
s_cmp_ge_u32 s88, s73
s_addc_u32 s89, s88, 0
s_lshl_b32 s89, s89, 7
s_cmp_eq_u32 s88, 7
s_cselect_b32 s87, s93, s89
s_and_b32 s33, s33, 0xffff
s_mov_b32 s34, 8388608
s_mov_b32 s35, 0x20000
s_mov_b32 s98, 0xffff0000
v_and_b32_e32 v2, 15, v0
v_mov_b32_e32 v56, 0x3f803f80
v_mov_b32_e32 v57, 0x3f803f80
v_mov_b32_e32 v58, 0x3f803f80
v_mov_b32_e32 v59, 0x3f803f80
v_and_b32_e32 v6, 7, v2
v_xor_b32_e32 v6, v6, v3
v_lshlrev_b32_e32 v6, 4, v6
v_lshl_add_u32 v15, v2, 7, v6
s_add_u32 s88, s96, 81920
v_add_u32_e32 v28, s88, v15
v_xor_b32_e32 v29, 2048, v28
v_xor_b32_e32 v30, 4096, v28
v_xor_b32_e32 v31, 6144, v28
v_xor_b32_e32 v32, 64, v28
v_xor_b32_e32 v33, 2112, v28
v_xor_b32_e32 v34, 4160, v28
v_xor_b32_e32 v35, 6208, v28
v_add_u32_e32 v36, 49152, v28
v_add_u32_e32 v37, 49152, v29
v_add_u32_e32 v38, 49152, v30
v_add_u32_e32 v39, 49152, v31
v_add_u32_e32 v40, 49152, v32
v_add_u32_e32 v41, 49152, v33
v_add_u32_e32 v42, 49152, v34
v_add_u32_e32 v43, 49152, v35
s_add_u32 s88, s99, 65536
v_add_u32_e32 v44, s88, v15
v_xor_b32_e32 v45, 64, v44
v_add_u32_e32 v46, 49152, v44
v_add_u32_e32 v47, 49152, v45
v_add_u32_e32 v48, 32768, v15
v_xor_b32_e32 v49, 64, v48
s_lshl_b32 s88, s75, 6
v_xor_b32_e32 v50, s88, v48
s_lshl_b32 s88, s74, 11
v_add_u32_e32 v50, s88, v50
v_lshrrev_b32_e32 v6, 3, v2
v_and_b32_e32 v7, 3, v2
v_lshl_add_u32 v6, v6, 2, v7
v_bfe_u32 v7, v2, 2, 1
v_lshl_add_u32 v7, v7, 4, v6
v_lshlrev_b32_e32 v7, 7, v7
v_add_u32_e32 v7, s99, v7
s_lshl_b32 s88, s75, 2
v_add_u32_e32 v8, s88, v3
v_xor_b32_e32 v8, v8, v6
v_lshl_add_u32 v8, v8, 4, v7
v_add_u32_e32 v51, 65536, v8
v_xor_b32_e32 v52, 4096, v51
s_and_b32 s88, s73, 3
s_lshl_b32 s88, s88, 1
v_lshrrev_b32_e32 v8, 1, v3
v_add_u32_e32 v8, s88, v8
v_xor_b32_e32 v8, v8, v6
v_lshl_add_u32 v8, v8, 4, v7
v_and_b32_e32 v9, 1, v3
v_lshl_add_u32 v8, v9, 3, v8
v_add_u32_e32 v53, 90112, v8
s_xor_b32 s88, s75, s74
s_lshl_b32 s89, s72, 1
s_add_u32 s88, s88, s89
s_lshl_b32 s88, s88, 18
v_lshl_add_u32 v24, v4, 12, v5
v_add_u32_e32 v24, s88, v24
v_add_u32_e32 v25, 31744, v24
v_add_u32_e32 v26, 63488, v24
v_add_u32_e32 v27, 95232, v24
s_lshl_b32 s88, s16, 8
s_add_u32 s88, s88, s17
v_and_b32_e32 v6, 7, v0
v_lshlrev_b32_e32 v6, 4, v6
v_lshrrev_b32_e32 v54, 2, v0
s_add_u32 s92, s99, 114688
v_lshl_add_u32 v54, v54, 7, s92
v_and_b32_e32 v7, 3, v0
s_mov_b32 s39, 0x20000
s_mov_b32 s38, 1966080
s_cmp_ge_u32 s88, 480
s_cbranch_scc1 Ljw2_%=
s_mov_b32 s36, s58
s_and_b32 s37, s59, 0xffff
s_mul_i32 s89, s88, 2731
s_lshr_b32 s89, s89, 16
s_mul_i32 s92, s89, 24
s_sub_u32 s90, s88, s92
s_mul_i32 s92, s89, 98304
s_lshl_b32 s93, s90, 7
s_add_u32 s40, s92, s93
v_lshrrev_b32_e32 v8, 5, v0
v_lshlrev_b32_e32 v8, 2, v8
s_cmp_lt_u32 s89, 16
s_cselect_b32 s92, -1, 0
s_cselect_b32 s41, 16, 8
s_cselect_b32 s42, 4, 16
s_cselect_b32 s43, 20, 24
v_and_b32_e32 v8, s92, v8
v_add_u32_e32 v8, v8, v4
s_movk_i32 s76, 3072
s_lshl_b32 s92, s90, 2
s_add_u32 s92, s92, 0
s_mul_i32 s93, s92, 683
s_lshr_b32 s93, s93, 11
s_lshl_b32 s92, s92, 10
s_mul_i32 s93, s93, 58368
s_add_u32 s44, s92, s93
s_lshl_b32 s92, s90, 2
s_add_u32 s92, s92, 1
s_mul_i32 s93, s92, 683
s_lshr_b32 s93, s93, 11
s_lshl_b32 s92, s92, 10
s_mul_i32 s93, s93, 58368
s_add_u32 s45, s92, s93
s_lshl_b32 s92, s90, 2
s_add_u32 s92, s92, 2
s_mul_i32 s93, s92, 683
s_lshr_b32 s93, s93, 11
s_lshl_b32 s92, s92, 10
s_mul_i32 s93, s93, 58368
s_add_u32 s46, s92, s93
s_lshl_b32 s92, s90, 2
s_add_u32 s92, s92, 3
s_mul_i32 s93, s92, 683
s_lshr_b32 s93, s93, 11
s_lshl_b32 s92, s92, 10
s_mul_i32 s93, s93, 58368
)A"
R"A(s_add_u32 s47, s92, s93
s_mul_i32 s92, s89, 3072
s_add_u32 s92, s92, 16908288
s_mov_b32 s93, 0
s_branch Ljend_%=
Ljw2_%=:
s_mov_b32 s36, s62
s_and_b32 s37, s63, 0xffff
s_sub_u32 s88, s88, 480
s_and_b32 s90, s88, 7
s_sub_u32 s92, s88, 96
s_lshr_b32 s93, s92, 5
s_mul_i32 s93, s93, 12
s_bfe_u32 s92, s92, 0x20003
s_add_u32 s89, s92, s93
s_lshr_b32 s93, s17, 6
s_mul_i32 s93, s93, 12
s_bfe_u32 s92, s17, 0x30003
s_add_u32 s92, s92, s93
s_add_u32 s92, s92, 4
s_cmp_eq_u32 s16, 3
s_cselect_b32 s89, s92, s89
s_lshr_b32 s92, s88, 1
s_and_b32 s93, s88, 1
s_add_u32 s93, s93, 8
s_cmp_lt_u32 s88, 96
s_cselect_b32 s89, s92, s89
s_cselect_b32 s90, s93, s90
s_mul_i32 s92, s89, 43
s_lshr_b32 s92, s92, 7
s_mul_i32 s93, s92, 3
s_sub_u32 s93, s89, s93
s_mul_i32 s92, s92, 96
s_cmp_eq_u32 s93, 1
s_cselect_b32 s76, 48, 0
s_cmp_eq_u32 s93, 2
s_cselect_b32 s76, 32, s76
s_cselect_b32 s42, 48, 16
s_cselect_b32 s43, 56, 24
s_mov_b32 s41, 8
s_add_u32 s92, s92, s76
s_mul_i32 s92, s92, 1280
s_lshl_b32 s93, s90, 7
s_add_u32 s40, s92, s93
v_mov_b32_e32 v8, v4
s_movk_i32 s76, 1280
s_lshl_b32 s92, s90, 2
s_sub_u32 s92, s92, 32
s_cmp_lt_u32 s90, 8
s_cselect_b32 s92, s90, s92
s_cselect_b32 s45, 1, 240
s_cselect_b32 s44, 0, 4096
s_cselect_b32 s93, 1, 0
s_lshl_b32 s45, s45, 10
s_lshl_b32 s46, s45, 1
s_mul_i32 s47, s45, 3
s_add_u32 s45, s45, s44
s_add_u32 s46, s46, s44
s_add_u32 s47, s47, s44
s_mul_i32 s92, s92, 48
s_add_u32 s92, s92, s89
s_mul_i32 s92, s92, 5120
s_add_u32 s92, s92, 18874368
s_cmp_ge_u32 s88, 224
s_cselect_b32 s92, 0x40000000, s92
s_sub_u32 s88, s88, 224
s_cmp_lt_u32 s88, 64
s_cselect_b32 s38, 0, s38
Ljend_%=:
s_add_u32 s44, s44, s92
s_add_u32 s45, s45, s92
s_add_u32 s46, s46, s92
s_add_u32 s47, s47, s92
s_mul_i32 s41, s41, s76
s_add_u32 s41, s41, s40
s_sub_u32 s41, s41, 1024
s_mul_i32 s42, s42, s76
s_add_u32 s42, s42, s40
s_sub_u32 s42, s42, 2048
s_mul_i32 s43, s43, s76
s_add_u32 s43, s43, s40
s_sub_u32 s43, s43, 3072
v_mad_u32_u24 v207, v8, s76, v6
s_add_u32 s92, s93, 3
v_lshlrev_b32_e32 v7, s92, v7
v_add_u32_e32 v54, v54, v7
s_cmp_eq_u32 s93, 0
s_cselect_b32 s92, 32, 8
v_add_u32_e32 v55, s92, v54
s_mov_b64 s[10:11], s[12:13]
s_lshl_b32 s88, s72, 2
s_mul_i32 s89, s18, 2621440
s_add_u32 s56, s88, s16
s_xor_b32 s58, s56, 1
s_mul_i32 s56, s56, 40960
s_mul_i32 s58, s58, 40960
s_add_u32 s56, s56, s89
s_add_u32 s58, s58, s89
s_lshl_b32 s88, s73, 10
s_add_u32 s13, s56, s88
s_add_u32 s13, s13, 5406720
s_lshl_b32 s88, s73, 1
s_add_u32 s88, s88, s75
s_lshl_b32 s88, s88, 10
s_add_u32 s88, s88, 5373952
s_add_u32 s56, s56, s88
s_add_u32 s58, s58, s88
s_add_u32 s57, s56, 16384
s_add_u32 s59, s58, 16384
s_and_b32 s88, s19, 0
s_lshl_b32 s88, s88, 2
s_add_u32 s88, s88, s73
s_lshl_b32 s55, s88, 1
s_lshl_b32 s12, s74, 11
s_and_b32 s88, s17, 3
s_mul_i32 s88, s88, 2
s_add_u32 s88, s88, s73
s_and_b32 s19, s88, 7
v_lshlrev_b32_e32 v246, 3, v2
v_xor_b32_e32 v6, s16, v3
v_lshlrev_b32_e32 v6, 2, v6
v_lshl_add_u32 v6, v3, 8, v6
v_lshl_add_u32 v248, v2, 4, v6
s_lshl_b32 s88, s16, 2
v_lshl_add_u32 v249, v2, 4, s88
s_lshl_b32 s88, s16, 8
v_lshl_add_u32 v250, v2, 4, s88
v_lshlrev_b32_e32 v251, 4, v2
v_mul_u32_u24_e32 v247, 80, v3
v_mov_b32_e32 v10, 0x3acccccd
v_mov_b32_e32 v12, 0x3727c5ac
s_add_u32 m0, s99, 90112
s_nop 0
buffer_load_dwordx4 v16, s[24:27], s79 offen offset:0 lds
buffer_load_dwordx4 v17, s[24:27], s79 offen offset:1024 lds
buffer_load_dwordx4 v18, s[24:27], s79 offen offset:2048 lds
buffer_load_dwordx4 v19, s[24:27], s79 offen offset:3072 lds
s_add_u32 m0, s96, 131072
s_nop 0
buffer_load_dwordx4 v20, s[28:31], s80 offen offset:0 lds
buffer_load_dwordx4 v21, s[28:31], s80 offen offset:1024 lds
buffer_load_dwordx2 v[204:205], off, s[20:23], 0 sc1
s_waitcnt vmcnt(9)
s_barrier
ds_read_b128 v[60:63], v44 offset:0
ds_read_b128 v[64:67], v44 offset:2048
ds_read_b128 v[68:71], v28 offset:0
ds_read_b128 v[72:75], v29 offset:0
ds_read_b128 v[76:79], v30 offset:0
ds_read_b128 v[80:83], v31 offset:0
ds_read_b128 v[84:87], v48 offset:0
s_bitcmp1_b32 s77, 0
s_cbranch_scc1 Lp1o_%=
s_waitcnt lgkmcnt(6)
v_mfma_f32_16x16x32_bf16 v[168:171], v[60:63], v[60:63], 0
ds_read_b128 v[88:91], v45 offset:0
ds_read_b128 v[196:199], v52 offset:1024
v_mfma_f32_16x16x32_bf16 v[160:163], v[60:63], v[56:59], 0
ds_read_b128 v[92:95], v45 offset:2048
s_waitcnt lgkmcnt(8)
v_mfma_f32_16x16x32_bf16 v[172:175], v[64:67], v[64:67], 0
ds_read_b128 v[96:99], v32 offset:0
v_mfma_f32_16x16x32_bf16 v[164:167], v[64:67], v[56:59], 0
ds_read_b128 v[100:103], v33 offset:0
s_waitcnt lgkmcnt(9)
v_mfma_f32_16x16x32_bf16 v[176:179], v[56:59], v[68:71], 0
ds_read_b128 v[104:107], v34 offset:0
v_mfma_f32_16x16x32_bf16 v[120:123], v[60:63], v[68:71], 0
ds_read_b128 v[108:111], v35 offset:0
s_add_u32 m0, s96, 155648
v_mfma_f32_16x16x32_bf16 v[124:127], v[64:67], v[68:71], 0
buffer_load_dwordx4 v20, s[28:31], s81 offen offset:0 lds
s_waitcnt lgkmcnt(10)
v_mfma_f32_16x16x32_bf16 v[128:131], v[60:63], v[72:75], 0
ds_read_b128 v[112:115], v49 offset:0
v_mfma_f32_16x16x32_bf16 v[132:135], v[64:67], v[72:75], 0
buffer_load_dwordx4 v21, s[28:31], s81 offen offset:1024 lds
s_waitcnt lgkmcnt(10)
v_mfma_f32_16x16x32_bf16 v[136:139], v[60:63], v[76:79], 0
ds_read_b128 v[184:187], v51 offset:0
s_add_u32 m0, s97, 36864
s_cmp_eq_u32 s74, 0
s_cselect_b32 s91, s80, s81
v_mfma_f32_16x16x32_bf16 v[140:143], v[64:67], v[76:79], 0
buffer_load_dwordx4 v22, s[28:31], s91 offen offset:0 lds
s_waitcnt lgkmcnt(10)
v_mfma_f32_16x16x32_bf16 v[144:147], v[60:63], v[80:83], 0
ds_read_b128 v[188:191], v51 offset:1024
s_add_u32 m0, s99, 114688
v_mfma_f32_16x16x32_bf16 v[148:151], v[64:67], v[80:83], 0
buffer_load_dwordx4 v16, s[24:27], s80 offen offset:0 lds
s_waitcnt lgkmcnt(10)
v_mfma_f32_16x16x32_bf16 v[152:155], v[60:63], v[84:87], 0
ds_read_b128 v[192:195], v52 offset:0
v_mfma_f32_16x16x32_bf16 v[156:159], v[64:67], v[84:87], 0
buffer_load_dwordx4 v17, s[24:27], s80 offen offset:1024 lds
s_waitcnt vmcnt(8) lgkmcnt(0)
s_barrier
v_mfma_f32_16x16x32_bf16 v[168:171], v[88:91], v[88:91], v[168:171]
ds_read_b128 v[60:63], v44 offset:24576
v_mfma_f32_16x16x32_bf16 v[160:163], v[88:91], v[56:59], v[160:163]
ds_read_b128 v[64:67], v44 offset:26624
v_mfma_f32_16x16x32_bf16 v[172:175], v[92:95], v[92:95], v[172:175]
ds_read_b128 v[68:71], v28 offset:24576
v_mfma_f32_16x16x32_bf16 v[164:167], v[92:95], v[56:59], v[164:167]
ds_read_b128 v[72:75], v29 offset:24576
v_mfma_f32_16x16x32_bf16 v[176:179], v[56:59], v[96:99], v[176:179]
ds_read_b128 v[76:79], v30 offset:24576
v_mfma_f32_16x16x32_bf16 v[120:123], v[88:91], v[96:99], v[120:123]
ds_read_b128 v[80:83], v31 offset:24576
v_mfma_f32_16x16x32_bf16 v[124:127], v[92:95], v[96:99], v[124:127]
buffer_load_dwordx4 v18, s[24:27], s80 offen offset:2048 lds
v_mfma_f32_16x16x32_bf16 v[128:131], v[88:91], v[100:103], v[128:131]
ds_read_b128 v[84:87], v48 offset:2048
v_mfma_f32_16x16x32_bf16 v[132:135], v[92:95], v[100:103], v[132:135]
buffer_load_dwordx4 v19, s[24:27], s80 offen offset:3072 lds
v_mfma_f32_16x16x32_bf16 v[136:139], v[88:91], v[104:107], v[136:139]
s_add_u32 m0, s99, 139264
v_mfma_f32_16x16x32_bf16 v[140:143], v[92:95], v[104:107], v[140:143]
buffer_load_dwordx4 v16, s[24:27], s81 offen offset:0 lds
v_mfma_f32_16x16x32_bf16 v[144:147], v[88:91], v[108:111], v[144:147]
v_mfma_f32_16x16x32_bf16 v[148:151], v[92:95], v[108:111], v[148:151]
buffer_load_dwordx4 v17, s[24:27], s81 offen offset:1024 lds
v_mfma_f32_16x16x32_bf16 v[152:155], v[88:91], v[112:115], v[152:155]
v_mfma_f32_16x16x32_bf16 v[156:159], v[92:95], v[112:115], v[156:159]
buffer_load_dwordx4 v18, s[24:27], s81 offen offset:2048 lds
s_waitcnt lgkmcnt(6)
v_mfma_f32_16x16x32_bf16 v[168:171], v[60:63], v[60:63], v[168:171]
ds_read_b128 v[88:91], v45 offset:24576
v_mfma_f32_16x16x32_bf16 v[160:163], v[60:63], v[56:59], v[160:163]
ds_read_b128 v[92:95], v45 offset:26624
s_waitcnt lgkmcnt(7)
v_mfma_f32_16x16x32_bf16 v[172:175], v[64:67], v[64:67], v[172:175]
ds_read_b128 v[96:99], v32 offset:24576
v_mfma_f32_16x16x32_bf16 v[164:167], v[64:67], v[56:59], v[164:167]
ds_read_b128 v[100:103], v33 offset:24576
)A"
R"A(s_waitcnt lgkmcnt(8)
v_mfma_f32_16x16x32_bf16 v[176:179], v[56:59], v[68:71], v[176:179]
ds_read_b128 v[104:107], v34 offset:24576
v_mfma_f32_16x16x32_bf16 v[120:123], v[60:63], v[68:71], v[120:123]
ds_read_b128 v[108:111], v35 offset:24576
v_mfma_f32_16x16x32_bf16 v[124:127], v[64:67], v[68:71], v[124:127]
buffer_load_dwordx4 v19, s[24:27], s81 offen offset:3072 lds
s_waitcnt lgkmcnt(9)
v_mfma_f32_16x16x32_bf16 v[128:131], v[60:63], v[72:75], v[128:131]
ds_read_b128 v[112:115], v49 offset:2048
s_add_u32 m0, s99, 65536
v_mfma_f32_16x16x32_bf16 v[132:135], v[64:67], v[72:75], v[132:135]
buffer_load_dwordx4 v16, s[24:27], s82 offen offset:0 lds
s_waitcnt lgkmcnt(9)
v_mfma_f32_16x16x32_bf16 v[136:139], v[60:63], v[76:79], v[136:139]
ds_read_b128 v[116:119], v50 offset:0
v_mfma_f32_16x16x32_bf16 v[140:143], v[64:67], v[76:79], v[140:143]
buffer_load_dwordx4 v17, s[24:27], s82 offen offset:1024 lds
s_waitcnt lgkmcnt(9)
v_mfma_f32_16x16x32_bf16 v[144:147], v[60:63], v[80:83], v[144:147]
ds_read_b64 v[200:201], v53 offset:0
v_mfma_f32_16x16x32_bf16 v[148:151], v[64:67], v[80:83], v[148:151]
buffer_load_dwordx4 v18, s[24:27], s82 offen offset:2048 lds
s_waitcnt lgkmcnt(9)
v_mfma_f32_16x16x32_bf16 v[152:155], v[60:63], v[84:87], v[152:155]
ds_read_b64 v[202:203], v53 offset:1024
v_mfma_f32_16x16x32_bf16 v[156:159], v[64:67], v[84:87], v[156:159]
buffer_load_dwordx4 v19, s[24:27], s82 offen offset:3072 lds
s_waitcnt vmcnt(8) lgkmcnt(0)
s_barrier
v_mfma_f32_16x16x32_bf16 v[180:183], v[56:59], v[116:119], 0
ds_read_b128 v[60:63], v46 offset:0
v_mfma_f32_16x16x32_bf16 v[168:171], v[88:91], v[88:91], v[168:171]
ds_read_b128 v[64:67], v46 offset:2048
v_mfma_f32_16x16x32_bf16 v[160:163], v[88:91], v[56:59], v[160:163]
ds_read_b128 v[68:71], v36 offset:0
v_mfma_f32_16x16x32_bf16 v[172:175], v[92:95], v[92:95], v[172:175]
ds_read_b128 v[72:75], v37 offset:0
v_mfma_f32_16x16x32_bf16 v[164:167], v[92:95], v[56:59], v[164:167]
ds_read_b128 v[76:79], v38 offset:0
v_mfma_f32_16x16x32_bf16 v[176:179], v[56:59], v[96:99], v[176:179]
ds_read_b128 v[80:83], v39 offset:0
v_mfma_f32_16x16x32_bf16 v[120:123], v[88:91], v[96:99], v[120:123]
ds_read_b128 v[84:87], v48 offset:4096
s_add_u32 m0, s96, 81920
v_mfma_f32_16x16x32_bf16 v[124:127], v[92:95], v[96:99], v[124:127]
buffer_load_dwordx4 v20, s[28:31], s82 offen offset:0 lds
v_mfma_f32_16x16x32_bf16 v[128:131], v[88:91], v[100:103], v[128:131]
v_mfma_f32_16x16x32_bf16 v[132:135], v[92:95], v[100:103], v[132:135]
buffer_load_dwordx4 v21, s[28:31], s82 offen offset:1024 lds
v_mfma_f32_16x16x32_bf16 v[136:139], v[88:91], v[104:107], v[136:139]
s_add_u32 m0, s97, 40960
s_cmp_eq_u32 s74, 0
s_cselect_b32 s91, s82, s83
v_mfma_f32_16x16x32_bf16 v[140:143], v[92:95], v[104:107], v[140:143]
buffer_load_dwordx4 v22, s[28:31], s91 offen offset:0 lds
v_mfma_f32_16x16x32_bf16 v[144:147], v[88:91], v[108:111], v[144:147]
s_add_u32 m0, s99, 90112
v_mfma_f32_16x16x32_bf16 v[148:151], v[92:95], v[108:111], v[148:151]
buffer_load_dwordx4 v16, s[24:27], s83 offen offset:0 lds
v_mfma_f32_16x16x32_bf16 v[152:155], v[88:91], v[112:115], v[152:155]
v_mfma_f32_16x16x32_bf16 v[156:159], v[92:95], v[112:115], v[156:159]
buffer_load_dwordx4 v17, s[24:27], s83 offen offset:1024 lds
s_waitcnt lgkmcnt(6)
v_mfma_f32_16x16x32_bf16 v[168:171], v[60:63], v[60:63], v[168:171]
ds_read_b128 v[88:91], v47 offset:0
v_readfirstlane_b32 s70, v204
v_readfirstlane_b32 s71, v205
v_mfma_f32_16x16x32_bf16 v[160:163], v[60:63], v[56:59], v[160:163]
ds_read_b128 v[92:95], v47 offset:2048
s_waitcnt lgkmcnt(7)
v_mfma_f32_16x16x32_bf16 v[172:175], v[64:67], v[64:67], v[172:175]
ds_read_b128 v[96:99], v40 offset:0
s_add_u32 s70, s70, 1
s_addc_u32 s71, s71, 0
v_mfma_f32_16x16x32_bf16 v[164:167], v[64:67], v[56:59], v[164:167]
ds_read_b128 v[100:103], v41 offset:0
s_waitcnt lgkmcnt(8)
v_mfma_f32_16x16x32_bf16 v[176:179], v[56:59], v[68:71], v[176:179]
ds_read_b128 v[104:107], v42 offset:0
v_mfma_f32_16x16x32_bf16 v[120:123], v[60:63], v[68:71], v[120:123]
ds_read_b128 v[108:111], v43 offset:0
v_mfma_f32_16x16x32_bf16 v[124:127], v[64:67], v[68:71], v[124:127]
ds_read_b128 v[112:115], v49 offset:4096
s_waitcnt lgkmcnt(10)
v_mfma_f32_16x16x32_bf16 v[128:131], v[60:63], v[72:75], v[128:131]
v_mfma_f32_16x16x32_bf16 v[132:135], v[64:67], v[72:75], v[132:135]
buffer_load_dwordx4 v18, s[24:27], s83 offen offset:2048 lds
s_waitcnt lgkmcnt(9)
v_mfma_f32_16x16x32_bf16 v[136:139], v[60:63], v[76:79], v[136:139]
v_mfma_f32_16x16x32_bf16 v[140:143], v[64:67], v[76:79], v[140:143]
buffer_load_dwordx4 v19, s[24:27], s83 offen offset:3072 lds
s_waitcnt lgkmcnt(8)
v_mfma_f32_16x16x32_bf16 v[144:147], v[60:63], v[80:83], v[144:147]
s_add_u32 m0, s96, 106496
v_mfma_f32_16x16x32_bf16 v[148:151], v[64:67], v[80:83], v[148:151]
buffer_load_dwordx4 v20, s[28:31], s83 offen offset:0 lds
s_waitcnt lgkmcnt(7)
v_mfma_f32_16x16x32_bf16 v[152:155], v[60:63], v[84:87], v[152:155]
v_mfma_f32_16x16x32_bf16 v[156:159], v[64:67], v[84:87], v[156:159]
buffer_load_dwordx4 v21, s[28:31], s83 offen offset:1024 lds
s_waitcnt vmcnt(13) lgkmcnt(0)
s_barrier
v_mfma_f32_16x16x32_bf16 v[168:171], v[88:91], v[88:91], v[168:171]
ds_read_b128 v[60:63], v46 offset:24576
v_mfma_f32_16x16x32_bf16 v[160:163], v[88:91], v[56:59], v[160:163]
ds_read_b128 v[64:67], v46 offset:26624
v_mfma_f32_16x16x32_bf16 v[172:175], v[92:95], v[92:95], v[172:175]
ds_read_b128 v[68:71], v36 offset:24576
v_mfma_f32_16x16x32_bf16 v[164:167], v[92:95], v[56:59], v[164:167]
ds_read_b128 v[72:75], v37 offset:24576
v_mfma_f32_16x16x32_bf16 v[176:179], v[56:59], v[96:99], v[176:179]
ds_read_b128 v[76:79], v38 offset:24576
v_mfma_f32_16x16x32_bf16 v[120:123], v[88:91], v[96:99], v[120:123]
ds_read_b128 v[80:83], v39 offset:24576
s_add_u32 m0, s99, 114688
v_mfma_f32_16x16x32_bf16 v[124:127], v[92:95], v[96:99], v[124:127]
buffer_load_dwordx4 v16, s[24:27], s84 offen offset:0 lds
v_mfma_f32_16x16x32_bf16 v[128:131], v[88:91], v[100:103], v[128:131]
ds_read_b128 v[84:87], v48 offset:6144
v_mfma_f32_16x16x32_bf16 v[132:135], v[92:95], v[100:103], v[132:135]
buffer_load_dwordx4 v17, s[24:27], s84 offen offset:1024 lds
v_mfma_f32_16x16x32_bf16 v[136:139], v[88:91], v[104:107], v[136:139]
v_mfma_f32_16x16x32_bf16 v[140:143], v[92:95], v[104:107], v[140:143]
buffer_load_dwordx4 v18, s[24:27], s84 offen offset:2048 lds
v_mfma_f32_16x16x32_bf16 v[144:147], v[88:91], v[108:111], v[144:147]
v_mfma_f32_16x16x32_bf16 v[148:151], v[92:95], v[108:111], v[148:151]
buffer_load_dwordx4 v19, s[24:27], s84 offen offset:3072 lds
v_mfma_f32_16x16x32_bf16 v[152:155], v[88:91], v[112:115], v[152:155]
s_add_u32 m0, s96, 131072
v_mfma_f32_16x16x32_bf16 v[156:159], v[92:95], v[112:115], v[156:159]
buffer_load_dwordx4 v20, s[28:31], s84 offen offset:0 lds
s_waitcnt lgkmcnt(6)
v_mfma_f32_16x16x32_bf16 v[168:171], v[60:63], v[60:63], v[168:171]
ds_read_b128 v[88:91], v47 offset:24576
v_mfma_f32_16x16x32_bf16 v[160:163], v[60:63], v[56:59], v[160:163]
ds_read_b128 v[92:95], v47 offset:26624
s_waitcnt lgkmcnt(7)
v_mfma_f32_16x16x32_bf16 v[172:175], v[64:67], v[64:67], v[172:175]
ds_read_b128 v[96:99], v40 offset:24576
v_mfma_f32_16x16x32_bf16 v[164:167], v[64:67], v[56:59], v[164:167]
ds_read_b128 v[100:103], v41 offset:24576
s_waitcnt lgkmcnt(8)
v_mfma_f32_16x16x32_bf16 v[176:179], v[56:59], v[68:71], v[176:179]
ds_read_b128 v[104:107], v42 offset:24576
v_mfma_f32_16x16x32_bf16 v[120:123], v[60:63], v[68:71], v[120:123]
ds_read_b128 v[108:111], v43 offset:24576
v_mfma_f32_16x16x32_bf16 v[124:127], v[64:67], v[68:71], v[124:127]
buffer_load_dwordx4 v21, s[28:31], s84 offen offset:1024 lds
s_waitcnt lgkmcnt(9)
v_mfma_f32_16x16x32_bf16 v[128:131], v[60:63], v[72:75], v[128:131]
ds_read_b128 v[112:115], v49 offset:6144
s_add_u32 m0, s97, 45056
s_cmp_eq_u32 s74, 0
s_cselect_b32 s91, s84, s85
v_mfma_f32_16x16x32_bf16 v[132:135], v[64:67], v[72:75], v[132:135]
buffer_load_dwordx4 v22, s[28:31], s91 offen offset:0 lds
s_waitcnt lgkmcnt(9)
v_mfma_f32_16x16x32_bf16 v[136:139], v[60:63], v[76:79], v[136:139]
)A"
R"A(ds_read_b128 v[116:119], v50 offset:4096
s_add_u32 m0, s94, 0
s_lshl_b32 s91, s19, 1
s_lshl_b32 s91, s91, 7
s_add_u32 s91, s91, s12
v_mfma_f32_16x16x32_bf16 v[140:143], v[64:67], v[76:79], v[140:143]
buffer_load_dwordx4 v24, s[32:35], s91 offen offset:0 lds
s_waitcnt lgkmcnt(9)
v_mfma_f32_16x16x32_bf16 v[144:147], v[60:63], v[80:83], v[144:147]
v_mfma_f32_16x16x32_bf16 v[148:151], v[64:67], v[80:83], v[148:151]
buffer_load_dwordx4 v25, s[32:35], s91 offen offset:1024 lds
s_waitcnt lgkmcnt(8)
v_mfma_f32_16x16x32_bf16 v[152:155], v[60:63], v[84:87], v[152:155]
v_mfma_f32_16x16x32_bf16 v[156:159], v[64:67], v[84:87], v[156:159]
buffer_load_dwordx4 v26, s[32:35], s91 offen offset:2048 lds
s_waitcnt vmcnt(16) lgkmcnt(0)
s_barrier
v_mfma_f32_16x16x32_bf16 v[180:183], v[56:59], v[116:119], v[180:183]
ds_read_b128 v[60:63], v44 offset:0
v_mfma_f32_16x16x32_bf16 v[168:171], v[88:91], v[88:91], v[168:171]
ds_read_b128 v[64:67], v44 offset:2048
v_mfma_f32_16x16x32_bf16 v[160:163], v[88:91], v[56:59], v[160:163]
ds_read_b128 v[68:71], v28 offset:0
v_mfma_f32_16x16x32_bf16 v[172:175], v[92:95], v[92:95], v[172:175]
ds_read_b128 v[72:75], v29 offset:0
v_mfma_f32_16x16x32_bf16 v[164:167], v[92:95], v[56:59], v[164:167]
ds_read_b128 v[76:79], v30 offset:0
v_mfma_f32_16x16x32_bf16 v[176:179], v[56:59], v[96:99], v[176:179]
ds_read_b128 v[80:83], v31 offset:0
v_mfma_f32_16x16x32_bf16 v[120:123], v[88:91], v[96:99], v[120:123]
ds_read_b128 v[84:87], v48 offset:8192
v_mfma_f32_16x16x32_bf16 v[124:127], v[92:95], v[96:99], v[124:127]
buffer_load_dwordx4 v27, s[32:35], s91 offen offset:3072 lds
v_mfma_f32_16x16x32_bf16 v[128:131], v[88:91], v[100:103], v[128:131]
s_add_u32 m0, s94, 4096
s_add_u32 s91, s91, 131072
v_mfma_f32_16x16x32_bf16 v[132:135], v[92:95], v[100:103], v[132:135]
buffer_load_dwordx4 v24, s[32:35], s91 offen offset:0 lds
v_mfma_f32_16x16x32_bf16 v[136:139], v[88:91], v[104:107], v[136:139]
v_mfma_f32_16x16x32_bf16 v[140:143], v[92:95], v[104:107], v[140:143]
buffer_load_dwordx4 v25, s[32:35], s91 offen offset:1024 lds
v_mfma_f32_16x16x32_bf16 v[144:147], v[88:91], v[108:111], v[144:147]
v_mfma_f32_16x16x32_bf16 v[148:151], v[92:95], v[108:111], v[148:151]
buffer_load_dwordx4 v26, s[32:35], s91 offen offset:2048 lds
v_mfma_f32_16x16x32_bf16 v[152:155], v[88:91], v[112:115], v[152:155]
v_mfma_f32_16x16x32_bf16 v[156:159], v[92:95], v[112:115], v[156:159]
buffer_load_dwordx4 v27, s[32:35], s91 offen offset:3072 lds
s_waitcnt lgkmcnt(6)
v_mfma_f32_16x16x32_bf16 v[168:171], v[60:63], v[60:63], v[168:171]
ds_read_b128 v[88:91], v45 offset:0
v_mfma_f32_16x16x32_bf16 v[160:163], v[60:63], v[56:59], v[160:163]
ds_read_b128 v[92:95], v45 offset:2048
s_waitcnt lgkmcnt(7)
v_mfma_f32_16x16x32_bf16 v[172:175], v[64:67], v[64:67], v[172:175]
ds_read_b128 v[96:99], v32 offset:0
v_mfma_f32_16x16x32_bf16 v[164:167], v[64:67], v[56:59], v[164:167]
ds_read_b128 v[100:103], v33 offset:0
s_waitcnt lgkmcnt(8)
v_mfma_f32_16x16x32_bf16 v[176:179], v[56:59], v[68:71], v[176:179]
ds_read_b128 v[104:107], v34 offset:0
v_mfma_f32_16x16x32_bf16 v[120:123], v[60:63], v[68:71], v[120:123]
ds_read_b128 v[108:111], v35 offset:0
s_add_u32 m0, s99, 139264
v_mfma_f32_16x16x32_bf16 v[124:127], v[64:67], v[68:71], v[124:127]
buffer_load_dwordx4 v16, s[24:27], s85 offen offset:0 lds
s_waitcnt lgkmcnt(9)
v_mfma_f32_16x16x32_bf16 v[128:131], v[60:63], v[72:75], v[128:131]
ds_read_b128 v[112:115], v49 offset:8192
v_mfma_f32_16x16x32_bf16 v[132:135], v[64:67], v[72:75], v[132:135]
buffer_load_dwordx4 v17, s[24:27], s85 offen offset:1024 lds
s_waitcnt lgkmcnt(9)
v_mfma_f32_16x16x32_bf16 v[136:139], v[60:63], v[76:79], v[136:139]
v_mfma_f32_16x16x32_bf16 v[140:143], v[64:67], v[76:79], v[140:143]
buffer_load_dwordx4 v18, s[24:27], s85 offen offset:2048 lds
s_waitcnt lgkmcnt(8)
v_mfma_f32_16x16x32_bf16 v[144:147], v[60:63], v[80:83], v[144:147]
v_mfma_f32_16x16x32_bf16 v[148:151], v[64:67], v[80:83], v[148:151]
buffer_load_dwordx4 v19, s[24:27], s85 offen offset:3072 lds
s_waitcnt lgkmcnt(7)
v_mfma_f32_16x16x32_bf16 v[152:155], v[60:63], v[84:87], v[152:155]
s_add_u32 m0, s96, 155648
v_mfma_f32_16x16x32_bf16 v[156:159], v[64:67], v[84:87], v[156:159]
buffer_load_dwordx4 v20, s[28:31], s85 offen offset:0 lds
s_waitcnt vmcnt(20) lgkmcnt(0)
s_barrier
v_mfma_f32_16x16x32_bf16 v[168:171], v[88:91], v[88:91], v[168:171]
ds_read_b128 v[60:63], v44 offset:24576
v_mfma_f32_16x16x32_bf16 v[160:163], v[88:91], v[56:59], v[160:163]
ds_read_b128 v[64:67], v44 offset:26624
v_mfma_f32_16x16x32_bf16 v[172:175], v[92:95], v[92:95], v[172:175]
ds_read_b128 v[68:71], v28 offset:24576
v_mfma_f32_16x16x32_bf16 v[164:167], v[92:95], v[56:59], v[164:167]
ds_read_b128 v[72:75], v29 offset:24576
v_mfma_f32_16x16x32_bf16 v[176:179], v[56:59], v[96:99], v[176:179]
ds_read_b128 v[76:79], v30 offset:24576
v_mfma_f32_16x16x32_bf16 v[120:123], v[88:91], v[96:99], v[120:123]
ds_read_b128 v[80:83], v31 offset:24576
v_mfma_f32_16x16x32_bf16 v[124:127], v[92:95], v[96:99], v[124:127]
buffer_load_dwordx4 v21, s[28:31], s85 offen offset:1024 lds
v_mfma_f32_16x16x32_bf16 v[128:131], v[88:91], v[100:103], v[128:131]
ds_read_b128 v[84:87], v48 offset:10240
s_add_u32 m0, s99, 65536
v_mfma_f32_16x16x32_bf16 v[132:135], v[92:95], v[100:103], v[132:135]
buffer_load_dwordx4 v16, s[24:27], s86 offen offset:0 lds
v_mfma_f32_16x16x32_bf16 v[136:139], v[88:91], v[104:107], v[136:139]
v_mfma_f32_16x16x32_bf16 v[140:143], v[92:95], v[104:107], v[140:143]
buffer_load_dwordx4 v17, s[24:27], s86 offen offset:1024 lds
v_mfma_f32_16x16x32_bf16 v[144:147], v[88:91], v[108:111], v[144:147]
v_mfma_f32_16x16x32_bf16 v[148:151], v[92:95], v[108:111], v[148:151]
buffer_load_dwordx4 v18, s[24:27], s86 offen offset:2048 lds
v_mfma_f32_16x16x32_bf16 v[152:155], v[88:91], v[112:115], v[152:155]
v_mfma_f32_16x16x32_bf16 v[156:159], v[92:95], v[112:115], v[156:159]
buffer_load_dwordx4 v19, s[24:27], s86 offen offset:3072 lds
s_waitcnt lgkmcnt(6)
v_mfma_f32_16x16x32_bf16 v[168:171], v[60:63], v[60:63], v[168:171]
ds_read_b128 v[88:91], v45 offset:24576
v_mfma_f32_16x16x32_bf16 v[160:163], v[60:63], v[56:59], v[160:163]
ds_read_b128 v[92:95], v45 offset:26624
s_waitcnt lgkmcnt(7)
v_mfma_f32_16x16x32_bf16 v[172:175], v[64:67], v[64:67], v[172:175]
ds_read_b128 v[96:99], v32 offset:24576
v_mfma_f32_16x16x32_bf16 v[164:167], v[64:67], v[56:59], v[164:167]
ds_read_b128 v[100:103], v33 offset:24576
s_waitcnt lgkmcnt(8)
v_mfma_f32_16x16x32_bf16 v[176:179], v[56:59], v[68:71], v[176:179]
ds_read_b128 v[104:107], v34 offset:24576
v_mfma_f32_16x16x32_bf16 v[120:123], v[60:63], v[68:71], v[120:123]
ds_read_b128 v[108:111], v35 offset:24576
v_mfma_f32_16x16x32_bf16 v[124:127], v[64:67], v[68:71], v[124:127]
ds_read_b128 v[112:115], v49 offset:10240
s_waitcnt lgkmcnt(10)
v_mfma_f32_16x16x32_bf16 v[128:131], v[60:63], v[72:75], v[128:131]
ds_read_b128 v[116:119], v50 offset:8192
v_mfma_f32_16x16x32_bf16 v[132:135], v[64:67], v[72:75], v[132:135]
s_waitcnt lgkmcnt(10)
v_mfma_f32_16x16x32_bf16 v[136:139], v[60:63], v[76:79], v[136:139]
s_add_u32 m0, s96, 81920
v_mfma_f32_16x16x32_bf16 v[140:143], v[64:67], v[76:79], v[140:143]
buffer_load_dwordx4 v20, s[28:31], s86 offen offset:0 lds
s_waitcnt lgkmcnt(9)
v_mfma_f32_16x16x32_bf16 v[144:147], v[60:63], v[80:83], v[144:147]
v_mfma_f32_16x16x32_bf16 v[148:151], v[64:67], v[80:83], v[148:151]
buffer_load_dwordx4 v21, s[28:31], s86 offen offset:1024 lds
s_waitcnt lgkmcnt(8)
v_mfma_f32_16x16x32_bf16 v[152:155], v[60:63], v[84:87], v[152:155]
s_add_u32 m0, s97, 49152
s_cmp_eq_u32 s74, 0
s_cselect_b32 s91, s86, s87
v_mfma_f32_16x16x32_bf16 v[156:159], v[64:67], v[84:87], v[156:159]
buffer_load_dwordx4 v22, s[28:31], s91 offen offset:0 lds
s_waitcnt vmcnt(21) lgkmcnt(0)
s_barrier
v_mfma_f32_16x16x32_bf16 v[180:183], v[56:59], v[116:119], v[180:183]
ds_read_b128 v[60:63], v46 offset:0
v_mfma_f32_16x16x32_bf16 v[168:171], v[88:91], v[88:91], v[168:171]
ds_read_b128 v[64:67], v46 offset:2048
v_mfma_f32_16x16x32_bf16 v[160:163], v[88:91], v[56:59], v[160:163]
)A"
R"A(ds_read_b128 v[68:71], v36 offset:0
v_mfma_f32_16x16x32_bf16 v[172:175], v[92:95], v[92:95], v[172:175]
ds_read_b128 v[72:75], v37 offset:0
v_mfma_f32_16x16x32_bf16 v[164:167], v[92:95], v[56:59], v[164:167]
ds_read_b128 v[76:79], v38 offset:0
v_mfma_f32_16x16x32_bf16 v[176:179], v[56:59], v[96:99], v[176:179]
ds_read_b128 v[80:83], v39 offset:0
v_mfma_f32_16x16x32_bf16 v[120:123], v[88:91], v[96:99], v[120:123]
ds_read_b128 v[84:87], v48 offset:12288
s_add_u32 m0, s99, 90112
v_mfma_f32_16x16x32_bf16 v[124:127], v[92:95], v[96:99], v[124:127]
buffer_load_dwordx4 v16, s[24:27], s87 offen offset:0 lds
v_mfma_f32_16x16x32_bf16 v[128:131], v[88:91], v[100:103], v[128:131]
v_mfma_f32_16x16x32_bf16 v[132:135], v[92:95], v[100:103], v[132:135]
buffer_load_dwordx4 v17, s[24:27], s87 offen offset:1024 lds
v_mfma_f32_16x16x32_bf16 v[136:139], v[88:91], v[104:107], v[136:139]
v_mfma_f32_16x16x32_bf16 v[140:143], v[92:95], v[104:107], v[140:143]
buffer_load_dwordx4 v18, s[24:27], s87 offen offset:2048 lds
v_mfma_f32_16x16x32_bf16 v[144:147], v[88:91], v[108:111], v[144:147]
v_mfma_f32_16x16x32_bf16 v[148:151], v[92:95], v[108:111], v[148:151]
buffer_load_dwordx4 v19, s[24:27], s87 offen offset:3072 lds
v_mfma_f32_16x16x32_bf16 v[152:155], v[88:91], v[112:115], v[152:155]
s_add_u32 m0, s96, 106496
v_mfma_f32_16x16x32_bf16 v[156:159], v[92:95], v[112:115], v[156:159]
buffer_load_dwordx4 v20, s[28:31], s87 offen offset:0 lds
s_waitcnt lgkmcnt(6)
v_mfma_f32_16x16x32_bf16 v[168:171], v[60:63], v[60:63], v[168:171]
ds_read_b128 v[88:91], v47 offset:0
v_mfma_f32_16x16x32_bf16 v[160:163], v[60:63], v[56:59], v[160:163]
ds_read_b128 v[92:95], v47 offset:2048
s_waitcnt lgkmcnt(7)
v_mfma_f32_16x16x32_bf16 v[172:175], v[64:67], v[64:67], v[172:175]
ds_read_b128 v[96:99], v40 offset:0
v_mfma_f32_16x16x32_bf16 v[164:167], v[64:67], v[56:59], v[164:167]
ds_read_b128 v[100:103], v41 offset:0
s_waitcnt lgkmcnt(8)
v_mfma_f32_16x16x32_bf16 v[176:179], v[56:59], v[68:71], v[176:179]
ds_read_b128 v[104:107], v42 offset:0
v_mfma_f32_16x16x32_bf16 v[120:123], v[60:63], v[68:71], v[120:123]
ds_read_b128 v[108:111], v43 offset:0
v_mfma_f32_16x16x32_bf16 v[124:127], v[64:67], v[68:71], v[124:127]
ds_read_b128 v[112:115], v49 offset:12288
s_waitcnt lgkmcnt(10)
v_mfma_f32_16x16x32_bf16 v[128:131], v[60:63], v[72:75], v[128:131]
v_mfma_f32_16x16x32_bf16 v[132:135], v[64:67], v[72:75], v[132:135]
s_waitcnt lgkmcnt(9)
v_mfma_f32_16x16x32_bf16 v[136:139], v[60:63], v[76:79], v[136:139]
v_mfma_f32_16x16x32_bf16 v[140:143], v[64:67], v[76:79], v[140:143]
s_waitcnt lgkmcnt(8)
v_mfma_f32_16x16x32_bf16 v[144:147], v[60:63], v[80:83], v[144:147]
v_mfma_f32_16x16x32_bf16 v[148:151], v[64:67], v[80:83], v[148:151]
s_waitcnt lgkmcnt(7)
v_mfma_f32_16x16x32_bf16 v[152:155], v[60:63], v[84:87], v[152:155]
v_mfma_f32_16x16x32_bf16 v[156:159], v[64:67], v[84:87], v[156:159]
buffer_load_dwordx4 v21, s[28:31], s87 offen offset:1024 lds
s_waitcnt vmcnt(13) lgkmcnt(0)
s_barrier
v_mfma_f32_16x16x32_bf16 v[168:171], v[88:91], v[88:91], v[168:171]
ds_read_b128 v[60:63], v46 offset:24576
v_mfma_f32_16x16x32_bf16 v[160:163], v[88:91], v[56:59], v[160:163]
ds_read_b128 v[64:67], v46 offset:26624
v_mfma_f32_16x16x32_bf16 v[172:175], v[92:95], v[92:95], v[172:175]
ds_read_b128 v[68:71], v36 offset:24576
v_mfma_f32_16x16x32_bf16 v[164:167], v[92:95], v[56:59], v[164:167]
ds_read_b128 v[72:75], v37 offset:24576
v_mfma_f32_16x16x32_bf16 v[176:179], v[56:59], v[96:99], v[176:179]
ds_read_b128 v[76:79], v38 offset:24576
v_mfma_f32_16x16x32_bf16 v[120:123], v[88:91], v[96:99], v[120:123]
ds_read_b128 v[80:83], v39 offset:24576
v_mfma_f32_16x16x32_bf16 v[124:127], v[92:95], v[96:99], v[124:127]
ds_read_b128 v[84:87], v48 offset:14336
v_mfma_f32_16x16x32_bf16 v[128:131], v[88:91], v[100:103], v[128:131]
s_add_u32 m0, s99, 114688
v_mfma_f32_16x16x32_bf16 v[132:135], v[92:95], v[100:103], v[132:135]
buffer_load_dwordx4 v207, s[36:39], s40 offen offset:0 lds
v_mfma_f32_16x16x32_bf16 v[136:139], v[88:91], v[104:107], v[136:139]
v_mfma_f32_16x16x32_bf16 v[140:143], v[92:95], v[104:107], v[140:143]
buffer_load_dwordx4 v207, s[36:39], s41 offen offset:1024 lds
v_mfma_f32_16x16x32_bf16 v[144:147], v[88:91], v[108:111], v[144:147]
v_mfma_f32_16x16x32_bf16 v[148:151], v[92:95], v[108:111], v[148:151]
buffer_load_dwordx4 v207, s[36:39], s42 offen offset:2048 lds
v_mfma_f32_16x16x32_bf16 v[152:155], v[88:91], v[112:115], v[152:155]
v_mfma_f32_16x16x32_bf16 v[156:159], v[92:95], v[112:115], v[156:159]
buffer_load_dwordx4 v207, s[36:39], s43 offen offset:3072 lds
s_waitcnt lgkmcnt(6)
v_mfma_f32_16x16x32_bf16 v[168:171], v[60:63], v[60:63], v[168:171]
ds_read_b128 v[88:91], v47 offset:24576
v_mfma_f32_16x16x32_bf16 v[160:163], v[60:63], v[56:59], v[160:163]
ds_read_b128 v[92:95], v47 offset:26624
s_waitcnt lgkmcnt(7)
v_mfma_f32_16x16x32_bf16 v[172:175], v[64:67], v[64:67], v[172:175]
ds_read_b128 v[96:99], v40 offset:24576
v_mfma_f32_16x16x32_bf16 v[164:167], v[64:67], v[56:59], v[164:167]
ds_read_b128 v[100:103], v41 offset:24576
s_waitcnt lgkmcnt(8)
v_mfma_f32_16x16x32_bf16 v[176:179], v[56:59], v[68:71], v[176:179]
ds_read_b128 v[104:107], v42 offset:24576
v_mfma_f32_16x16x32_bf16 v[120:123], v[60:63], v[68:71], v[120:123]
ds_read_b128 v[108:111], v43 offset:24576
v_mfma_f32_16x16x32_bf16 v[124:127], v[64:67], v[68:71], v[124:127]
ds_read_b128 v[112:115], v49 offset:14336
s_waitcnt lgkmcnt(10)
v_mfma_f32_16x16x32_bf16 v[128:131], v[60:63], v[72:75], v[128:131]
ds_read_b128 v[116:119], v50 offset:12288
v_mfma_f32_16x16x32_bf16 v[132:135], v[64:67], v[72:75], v[132:135]
s_waitcnt lgkmcnt(10)
v_mfma_f32_16x16x32_bf16 v[136:139], v[60:63], v[76:79], v[136:139]
v_mfma_f32_16x16x32_bf16 v[140:143], v[64:67], v[76:79], v[140:143]
s_waitcnt lgkmcnt(9)
v_mfma_f32_16x16x32_bf16 v[144:147], v[60:63], v[80:83], v[144:147]
v_mfma_f32_16x16x32_bf16 v[148:151], v[64:67], v[80:83], v[148:151]
s_waitcnt lgkmcnt(8)
v_mfma_f32_16x16x32_bf16 v[152:155], v[60:63], v[84:87], v[152:155]
v_mfma_f32_16x16x32_bf16 v[156:159], v[64:67], v[84:87], v[156:159]
s_waitcnt vmcnt(10) lgkmcnt(0)
s_barrier
v_mfma_f32_16x16x32_bf16 v[180:183], v[56:59], v[116:119], v[180:183]
ds_read_b128 v[60:63], v44 offset:0
v_mfma_f32_16x16x32_bf16 v[168:171], v[88:91], v[88:91], v[168:171]
ds_read_b128 v[64:67], v44 offset:2048
v_mfma_f32_16x16x32_bf16 v[160:163], v[88:91], v[56:59], v[160:163]
ds_read_b128 v[68:71], v28 offset:0
v_mfma_f32_16x16x32_bf16 v[172:175], v[92:95], v[92:95], v[172:175]
ds_read_b128 v[72:75], v29 offset:0
v_mfma_f32_16x16x32_bf16 v[164:167], v[92:95], v[56:59], v[164:167]
ds_read_b128 v[76:79], v30 offset:0
v_mfma_f32_16x16x32_bf16 v[176:179], v[56:59], v[96:99], v[176:179]
ds_read_b128 v[80:83], v31 offset:0
v_mfma_f32_16x16x32_bf16 v[120:123], v[88:91], v[96:99], v[120:123]
ds_read_b128 v[84:87], v48 offset:16384
v_mfma_f32_16x16x32_bf16 v[124:127], v[92:95], v[96:99], v[124:127]
v_mfma_f32_16x16x32_bf16 v[128:131], v[88:91], v[100:103], v[128:131]
v_mfma_f32_16x16x32_bf16 v[132:135], v[92:95], v[100:103], v[132:135]
v_mfma_f32_16x16x32_bf16 v[136:139], v[88:91], v[104:107], v[136:139]
v_mfma_f32_16x16x32_bf16 v[140:143], v[92:95], v[104:107], v[140:143]
v_mfma_f32_16x16x32_bf16 v[144:147], v[88:91], v[108:111], v[144:147]
v_mfma_f32_16x16x32_bf16 v[148:151], v[92:95], v[108:111], v[148:151]
v_mfma_f32_16x16x32_bf16 v[152:155], v[88:91], v[112:115], v[152:155]
v_mfma_f32_16x16x32_bf16 v[156:159], v[92:95], v[112:115], v[156:159]
s_waitcnt lgkmcnt(6)
v_mfma_f32_16x16x32_bf16 v[168:171], v[60:63], v[60:63], v[168:171]
ds_read_b128 v[88:91], v45 offset:0
v_mfma_f32_16x16x32_bf16 v[160:163], v[60:63], v[56:59], v[160:163]
ds_read_b128 v[92:95], v45 offset:2048
s_waitcnt lgkmcnt(7)
v_mfma_f32_16x16x32_bf16 v[172:175], v[64:67], v[64:67], v[172:175]
ds_read_b128 v[96:99], v32 offset:0
v_mfma_f32_16x16x32_bf16 v[164:167], v[64:67], v[56:59], v[164:167]
ds_read_b128 v[100:103], v33 offset:0
s_waitcnt lgkmcnt(8)
v_mfma_f32_16x16x32_bf16 v[176:179], v[56:59], v[68:71], v[176:179]
)A"
R"A(ds_read_b128 v[104:107], v34 offset:0
v_mfma_f32_16x16x32_bf16 v[120:123], v[60:63], v[68:71], v[120:123]
ds_read_b128 v[108:111], v35 offset:0
v_mfma_f32_16x16x32_bf16 v[124:127], v[64:67], v[68:71], v[124:127]
ds_read_b128 v[112:115], v49 offset:16384
s_waitcnt lgkmcnt(10)
v_mfma_f32_16x16x32_bf16 v[128:131], v[60:63], v[72:75], v[128:131]
v_mfma_f32_16x16x32_bf16 v[132:135], v[64:67], v[72:75], v[132:135]
s_waitcnt lgkmcnt(9)
v_mfma_f32_16x16x32_bf16 v[136:139], v[60:63], v[76:79], v[136:139]
v_mfma_f32_16x16x32_bf16 v[140:143], v[64:67], v[76:79], v[140:143]
s_waitcnt lgkmcnt(8)
v_mfma_f32_16x16x32_bf16 v[144:147], v[60:63], v[80:83], v[144:147]
v_mfma_f32_16x16x32_bf16 v[148:151], v[64:67], v[80:83], v[148:151]
s_waitcnt lgkmcnt(7)
v_mfma_f32_16x16x32_bf16 v[152:155], v[60:63], v[84:87], v[152:155]
v_mfma_f32_16x16x32_bf16 v[156:159], v[64:67], v[84:87], v[156:159]
s_waitcnt vmcnt(4) lgkmcnt(0)
s_barrier
v_mfma_f32_16x16x32_bf16 v[168:171], v[88:91], v[88:91], v[168:171]
ds_read_b128 v[60:63], v44 offset:24576
v_mfma_f32_16x16x32_bf16 v[160:163], v[88:91], v[56:59], v[160:163]
ds_read_b128 v[64:67], v44 offset:26624
v_mfma_f32_16x16x32_bf16 v[172:175], v[92:95], v[92:95], v[172:175]
ds_read_b128 v[68:71], v28 offset:24576
v_mfma_f32_16x16x32_bf16 v[164:167], v[92:95], v[56:59], v[164:167]
ds_read_b128 v[72:75], v29 offset:24576
v_mfma_f32_16x16x32_bf16 v[176:179], v[56:59], v[96:99], v[176:179]
ds_read_b128 v[76:79], v30 offset:24576
v_mfma_f32_16x16x32_bf16 v[120:123], v[88:91], v[96:99], v[120:123]
ds_read_b128 v[80:83], v31 offset:24576
v_mfma_f32_16x16x32_bf16 v[124:127], v[92:95], v[96:99], v[124:127]
ds_read_b128 v[84:87], v48 offset:18432
v_mfma_f32_16x16x32_bf16 v[128:131], v[88:91], v[100:103], v[128:131]
v_mfma_f32_16x16x32_bf16 v[132:135], v[92:95], v[100:103], v[132:135]
v_mfma_f32_16x16x32_bf16 v[136:139], v[88:91], v[104:107], v[136:139]
v_mfma_f32_16x16x32_bf16 v[140:143], v[92:95], v[104:107], v[140:143]
v_mfma_f32_16x16x32_bf16 v[144:147], v[88:91], v[108:111], v[144:147]
v_mfma_f32_16x16x32_bf16 v[148:151], v[92:95], v[108:111], v[148:151]
v_mfma_f32_16x16x32_bf16 v[152:155], v[88:91], v[112:115], v[152:155]
v_mfma_f32_16x16x32_bf16 v[156:159], v[92:95], v[112:115], v[156:159]
s_waitcnt lgkmcnt(6)
v_mfma_f32_16x16x32_bf16 v[168:171], v[60:63], v[60:63], v[168:171]
ds_read_b128 v[88:91], v45 offset:24576
v_mfma_f32_16x16x32_bf16 v[160:163], v[60:63], v[56:59], v[160:163]
ds_read_b128 v[92:95], v45 offset:26624
s_waitcnt lgkmcnt(7)
v_mfma_f32_16x16x32_bf16 v[172:175], v[64:67], v[64:67], v[172:175]
ds_read_b128 v[96:99], v32 offset:24576
v_mfma_f32_16x16x32_bf16 v[164:167], v[64:67], v[56:59], v[164:167]
ds_read_b128 v[100:103], v33 offset:24576
s_waitcnt lgkmcnt(8)
v_mfma_f32_16x16x32_bf16 v[176:179], v[56:59], v[68:71], v[176:179]
ds_read_b128 v[104:107], v34 offset:24576
v_mfma_f32_16x16x32_bf16 v[120:123], v[60:63], v[68:71], v[120:123]
ds_read_b128 v[108:111], v35 offset:24576
v_mfma_f32_16x16x32_bf16 v[124:127], v[64:67], v[68:71], v[124:127]
ds_read_b128 v[112:115], v49 offset:18432
s_waitcnt lgkmcnt(10)
v_mfma_f32_16x16x32_bf16 v[128:131], v[60:63], v[72:75], v[128:131]
ds_read_b128 v[116:119], v50 offset:16384
v_mfma_f32_16x16x32_bf16 v[132:135], v[64:67], v[72:75], v[132:135]
s_waitcnt lgkmcnt(10)
v_mfma_f32_16x16x32_bf16 v[136:139], v[60:63], v[76:79], v[136:139]
v_mfma_f32_16x16x32_bf16 v[140:143], v[64:67], v[76:79], v[140:143]
s_waitcnt lgkmcnt(9)
v_mfma_f32_16x16x32_bf16 v[144:147], v[60:63], v[80:83], v[144:147]
v_mfma_f32_16x16x32_bf16 v[148:151], v[64:67], v[80:83], v[148:151]
s_waitcnt lgkmcnt(8)
v_mfma_f32_16x16x32_bf16 v[152:155], v[60:63], v[84:87], v[152:155]
v_mfma_f32_16x16x32_bf16 v[156:159], v[64:67], v[84:87], v[156:159]
s_branch Lp1e_%=
Lp1o_%=:
s_waitcnt lgkmcnt(6)
v_mfma_f32_16x16x32_bf16 v[168:171], v[60:63], v[60:63], 0
ds_read_b128 v[88:91], v45 offset:0
ds_read_b128 v[196:199], v52 offset:1024
v_mfma_f32_16x16x32_bf16 v[160:163], v[60:63], v[56:59], 0
ds_read_b128 v[92:95], v45 offset:2048
s_waitcnt lgkmcnt(8)
v_mfma_f32_16x16x32_bf16 v[172:175], v[64:67], v[64:67], 0
ds_read_b128 v[96:99], v32 offset:0
v_mfma_f32_16x16x32_bf16 v[164:167], v[64:67], v[56:59], 0
ds_read_b128 v[100:103], v33 offset:0
s_waitcnt lgkmcnt(9)
v_mfma_f32_16x16x32_bf16 v[176:179], v[56:59], v[68:71], 0
ds_read_b128 v[104:107], v34 offset:0
v_mfma_f32_16x16x32_bf16 v[120:123], v[60:63], v[68:71], 0
ds_read_b128 v[108:111], v35 offset:0
s_add_u32 m0, s96, 155648
v_mfma_f32_16x16x32_bf16 v[124:127], v[64:67], v[68:71], 0
buffer_load_dwordx4 v20, s[28:31], s81 offen offset:0 lds
s_waitcnt lgkmcnt(10)
v_mfma_f32_16x16x32_bf16 v[128:131], v[60:63], v[72:75], 0
ds_read_b128 v[112:115], v49 offset:0
v_mfma_f32_16x16x32_bf16 v[132:135], v[64:67], v[72:75], 0
buffer_load_dwordx4 v21, s[28:31], s81 offen offset:1024 lds
s_waitcnt lgkmcnt(10)
v_mfma_f32_16x16x32_bf16 v[136:139], v[60:63], v[76:79], 0
ds_read_b128 v[184:187], v51 offset:0
s_add_u32 m0, s97, 36864
s_cmp_eq_u32 s74, 0
s_cselect_b32 s91, s80, s81
v_mfma_f32_16x16x32_bf16 v[140:143], v[64:67], v[76:79], 0
buffer_load_dwordx4 v22, s[28:31], s91 offen offset:0 lds
s_waitcnt lgkmcnt(10)
v_mfma_f32_16x16x32_bf16 v[144:147], v[60:63], v[80:83], 0
ds_read_b128 v[188:191], v51 offset:1024
s_add_u32 m0, s99, 114688
v_mfma_f32_16x16x32_bf16 v[148:151], v[64:67], v[80:83], 0
buffer_load_dwordx4 v16, s[24:27], s80 offen offset:0 lds
s_waitcnt lgkmcnt(10)
v_mfma_f32_16x16x32_bf16 v[152:155], v[60:63], v[84:87], 0
ds_read_b128 v[192:195], v52 offset:0
v_mfma_f32_16x16x32_bf16 v[156:159], v[64:67], v[84:87], 0
buffer_load_dwordx4 v17, s[24:27], s80 offen offset:1024 lds
s_waitcnt vmcnt(8) lgkmcnt(0)
s_barrier
v_mfma_f32_16x16x32_bf16 v[168:171], v[88:91], v[88:91], v[168:171]
ds_read_b128 v[60:63], v44 offset:24576
v_mfma_f32_16x16x32_bf16 v[160:163], v[88:91], v[56:59], v[160:163]
ds_read_b128 v[64:67], v44 offset:26624
v_mfma_f32_16x16x32_bf16 v[172:175], v[92:95], v[92:95], v[172:175]
ds_read_b128 v[68:71], v28 offset:24576
v_mfma_f32_16x16x32_bf16 v[164:167], v[92:95], v[56:59], v[164:167]
ds_read_b128 v[72:75], v29 offset:24576
v_mfma_f32_16x16x32_bf16 v[176:179], v[56:59], v[96:99], v[176:179]
ds_read_b128 v[76:79], v30 offset:24576
v_mfma_f32_16x16x32_bf16 v[120:123], v[88:91], v[96:99], v[120:123]
ds_read_b128 v[80:83], v31 offset:24576
v_mfma_f32_16x16x32_bf16 v[124:127], v[92:95], v[96:99], v[124:127]
buffer_load_dwordx4 v18, s[24:27], s80 offen offset:2048 lds
v_mfma_f32_16x16x32_bf16 v[128:131], v[88:91], v[100:103], v[128:131]
ds_read_b128 v[84:87], v48 offset:2048
v_mfma_f32_16x16x32_bf16 v[132:135], v[92:95], v[100:103], v[132:135]
buffer_load_dwordx4 v19, s[24:27], s80 offen offset:3072 lds
v_mfma_f32_16x16x32_bf16 v[136:139], v[88:91], v[104:107], v[136:139]
s_add_u32 m0, s99, 139264
v_mfma_f32_16x16x32_bf16 v[140:143], v[92:95], v[104:107], v[140:143]
buffer_load_dwordx4 v16, s[24:27], s81 offen offset:0 lds
v_mfma_f32_16x16x32_bf16 v[144:147], v[88:91], v[108:111], v[144:147]
v_mfma_f32_16x16x32_bf16 v[148:151], v[92:95], v[108:111], v[148:151]
buffer_load_dwordx4 v17, s[24:27], s81 offen offset:1024 lds
v_mfma_f32_16x16x32_bf16 v[152:155], v[88:91], v[112:115], v[152:155]
v_mfma_f32_16x16x32_bf16 v[156:159], v[92:95], v[112:115], v[156:159]
buffer_load_dwordx4 v18, s[24:27], s81 offen offset:2048 lds
s_waitcnt lgkmcnt(6)
v_mfma_f32_16x16x32_bf16 v[168:171], v[60:63], v[60:63], v[168:171]
ds_read_b128 v[88:91], v45 offset:24576
v_mfma_f32_16x16x32_bf16 v[160:163], v[60:63], v[56:59], v[160:163]
ds_read_b128 v[92:95], v45 offset:26624
s_waitcnt lgkmcnt(7)
v_mfma_f32_16x16x32_bf16 v[172:175], v[64:67], v[64:67], v[172:175]
ds_read_b128 v[96:99], v32 offset:24576
v_mfma_f32_16x16x32_bf16 v[164:167], v[64:67], v[56:59], v[164:167]
ds_read_b128 v[100:103], v33 offset:24576
s_waitcnt lgkmcnt(8)
v_mfma_f32_16x16x32_bf16 v[176:179], v[56:59], v[68:71], v[176:179]
ds_read_b128 v[104:107], v34 offset:24576
v_mfma_f32_16x16x32_bf16 v[120:123], v[60:63], v[68:71], v[120:123]
ds_read_b128 v[108:111], v35 offset:24576
)A"
R"A(v_mfma_f32_16x16x32_bf16 v[124:127], v[64:67], v[68:71], v[124:127]
buffer_load_dwordx4 v19, s[24:27], s81 offen offset:3072 lds
s_waitcnt lgkmcnt(9)
v_mfma_f32_16x16x32_bf16 v[128:131], v[60:63], v[72:75], v[128:131]
ds_read_b128 v[112:115], v49 offset:2048
s_add_u32 m0, s99, 65536
v_mfma_f32_16x16x32_bf16 v[132:135], v[64:67], v[72:75], v[132:135]
buffer_load_dwordx4 v16, s[24:27], s82 offen offset:0 lds
s_waitcnt lgkmcnt(9)
v_mfma_f32_16x16x32_bf16 v[136:139], v[60:63], v[76:79], v[136:139]
ds_read_b128 v[116:119], v50 offset:0
v_mfma_f32_16x16x32_bf16 v[140:143], v[64:67], v[76:79], v[140:143]
buffer_load_dwordx4 v17, s[24:27], s82 offen offset:1024 lds
s_waitcnt lgkmcnt(9)
v_mfma_f32_16x16x32_bf16 v[144:147], v[60:63], v[80:83], v[144:147]
ds_read_b64 v[200:201], v53 offset:0
v_mfma_f32_16x16x32_bf16 v[148:151], v[64:67], v[80:83], v[148:151]
buffer_load_dwordx4 v18, s[24:27], s82 offen offset:2048 lds
s_waitcnt lgkmcnt(9)
v_mfma_f32_16x16x32_bf16 v[152:155], v[60:63], v[84:87], v[152:155]
ds_read_b64 v[202:203], v53 offset:1024
v_mfma_f32_16x16x32_bf16 v[156:159], v[64:67], v[84:87], v[156:159]
buffer_load_dwordx4 v19, s[24:27], s82 offen offset:3072 lds
s_waitcnt vmcnt(8) lgkmcnt(0)
s_barrier
v_mfma_f32_16x16x32_bf16 v[180:183], v[56:59], v[116:119], 0
ds_read_b128 v[60:63], v46 offset:0
v_mfma_f32_16x16x32_bf16 v[168:171], v[88:91], v[88:91], v[168:171]
ds_read_b128 v[64:67], v46 offset:2048
v_mfma_f32_16x16x32_bf16 v[160:163], v[88:91], v[56:59], v[160:163]
ds_read_b128 v[68:71], v36 offset:0
v_mfma_f32_16x16x32_bf16 v[172:175], v[92:95], v[92:95], v[172:175]
ds_read_b128 v[72:75], v37 offset:0
v_mfma_f32_16x16x32_bf16 v[164:167], v[92:95], v[56:59], v[164:167]
ds_read_b128 v[76:79], v38 offset:0
v_mfma_f32_16x16x32_bf16 v[176:179], v[56:59], v[96:99], v[176:179]
ds_read_b128 v[80:83], v39 offset:0
v_mfma_f32_16x16x32_bf16 v[120:123], v[88:91], v[96:99], v[120:123]
ds_read_b128 v[84:87], v48 offset:4096
s_add_u32 m0, s96, 81920
v_mfma_f32_16x16x32_bf16 v[124:127], v[92:95], v[96:99], v[124:127]
buffer_load_dwordx4 v20, s[28:31], s82 offen offset:0 lds
v_mfma_f32_16x16x32_bf16 v[128:131], v[88:91], v[100:103], v[128:131]
v_mfma_f32_16x16x32_bf16 v[132:135], v[92:95], v[100:103], v[132:135]
buffer_load_dwordx4 v21, s[28:31], s82 offen offset:1024 lds
v_mfma_f32_16x16x32_bf16 v[136:139], v[88:91], v[104:107], v[136:139]
s_add_u32 m0, s97, 40960
s_cmp_eq_u32 s74, 0
s_cselect_b32 s91, s82, s83
v_mfma_f32_16x16x32_bf16 v[140:143], v[92:95], v[104:107], v[140:143]
buffer_load_dwordx4 v22, s[28:31], s91 offen offset:0 lds
v_mfma_f32_16x16x32_bf16 v[144:147], v[88:91], v[108:111], v[144:147]
s_add_u32 m0, s99, 90112
v_mfma_f32_16x16x32_bf16 v[148:151], v[92:95], v[108:111], v[148:151]
buffer_load_dwordx4 v16, s[24:27], s83 offen offset:0 lds
v_mfma_f32_16x16x32_bf16 v[152:155], v[88:91], v[112:115], v[152:155]
v_mfma_f32_16x16x32_bf16 v[156:159], v[92:95], v[112:115], v[156:159]
buffer_load_dwordx4 v17, s[24:27], s83 offen offset:1024 lds
s_waitcnt lgkmcnt(6)
v_mfma_f32_16x16x32_bf16 v[168:171], v[60:63], v[60:63], v[168:171]
ds_read_b128 v[88:91], v47 offset:0
v_readfirstlane_b32 s70, v204
v_readfirstlane_b32 s71, v205
v_mfma_f32_16x16x32_bf16 v[160:163], v[60:63], v[56:59], v[160:163]
ds_read_b128 v[92:95], v47 offset:2048
s_waitcnt lgkmcnt(7)
v_mfma_f32_16x16x32_bf16 v[172:175], v[64:67], v[64:67], v[172:175]
ds_read_b128 v[96:99], v40 offset:0
s_add_u32 s70, s70, 1
s_addc_u32 s71, s71, 0
v_mfma_f32_16x16x32_bf16 v[164:167], v[64:67], v[56:59], v[164:167]
ds_read_b128 v[100:103], v41 offset:0
s_waitcnt lgkmcnt(8)
v_mfma_f32_16x16x32_bf16 v[176:179], v[56:59], v[68:71], v[176:179]
ds_read_b128 v[104:107], v42 offset:0
v_mfma_f32_16x16x32_bf16 v[120:123], v[60:63], v[68:71], v[120:123]
ds_read_b128 v[108:111], v43 offset:0
v_mfma_f32_16x16x32_bf16 v[124:127], v[64:67], v[68:71], v[124:127]
ds_read_b128 v[112:115], v49 offset:4096
s_waitcnt lgkmcnt(10)
v_mfma_f32_16x16x32_bf16 v[128:131], v[60:63], v[72:75], v[128:131]
v_mfma_f32_16x16x32_bf16 v[132:135], v[64:67], v[72:75], v[132:135]
buffer_load_dwordx4 v18, s[24:27], s83 offen offset:2048 lds
s_waitcnt lgkmcnt(9)
v_mfma_f32_16x16x32_bf16 v[136:139], v[60:63], v[76:79], v[136:139]
v_mfma_f32_16x16x32_bf16 v[140:143], v[64:67], v[76:79], v[140:143]
buffer_load_dwordx4 v19, s[24:27], s83 offen offset:3072 lds
s_waitcnt lgkmcnt(8)
v_mfma_f32_16x16x32_bf16 v[144:147], v[60:63], v[80:83], v[144:147]
s_add_u32 m0, s96, 106496
v_mfma_f32_16x16x32_bf16 v[148:151], v[64:67], v[80:83], v[148:151]
buffer_load_dwordx4 v20, s[28:31], s83 offen offset:0 lds
s_waitcnt lgkmcnt(7)
v_mfma_f32_16x16x32_bf16 v[152:155], v[60:63], v[84:87], v[152:155]
v_mfma_f32_16x16x32_bf16 v[156:159], v[64:67], v[84:87], v[156:159]
buffer_load_dwordx4 v21, s[28:31], s83 offen offset:1024 lds
s_waitcnt vmcnt(13) lgkmcnt(0)
s_barrier
v_mfma_f32_16x16x32_bf16 v[168:171], v[88:91], v[88:91], v[168:171]
ds_read_b128 v[60:63], v46 offset:24576
v_mfma_f32_16x16x32_bf16 v[160:163], v[88:91], v[56:59], v[160:163]
ds_read_b128 v[64:67], v46 offset:26624
v_mfma_f32_16x16x32_bf16 v[172:175], v[92:95], v[92:95], v[172:175]
ds_read_b128 v[68:71], v36 offset:24576
v_mfma_f32_16x16x32_bf16 v[164:167], v[92:95], v[56:59], v[164:167]
ds_read_b128 v[72:75], v37 offset:24576
v_mfma_f32_16x16x32_bf16 v[176:179], v[56:59], v[96:99], v[176:179]
ds_read_b128 v[76:79], v38 offset:24576
v_mfma_f32_16x16x32_bf16 v[120:123], v[88:91], v[96:99], v[120:123]
ds_read_b128 v[80:83], v39 offset:24576
v_mfma_f32_16x16x32_bf16 v[124:127], v[92:95], v[96:99], v[124:127]
ds_read_b128 v[84:87], v48 offset:6144
v_mfma_f32_16x16x32_bf16 v[128:131], v[88:91], v[100:103], v[128:131]
s_add_u32 m0, s99, 114688
v_mfma_f32_16x16x32_bf16 v[132:135], v[92:95], v[100:103], v[132:135]
buffer_load_dwordx4 v16, s[24:27], s84 offen offset:0 lds
v_mfma_f32_16x16x32_bf16 v[136:139], v[88:91], v[104:107], v[136:139]
v_mfma_f32_16x16x32_bf16 v[140:143], v[92:95], v[104:107], v[140:143]
buffer_load_dwordx4 v17, s[24:27], s84 offen offset:1024 lds
v_mfma_f32_16x16x32_bf16 v[144:147], v[88:91], v[108:111], v[144:147]
v_mfma_f32_16x16x32_bf16 v[148:151], v[92:95], v[108:111], v[148:151]
buffer_load_dwordx4 v18, s[24:27], s84 offen offset:2048 lds
v_mfma_f32_16x16x32_bf16 v[152:155], v[88:91], v[112:115], v[152:155]
v_mfma_f32_16x16x32_bf16 v[156:159], v[92:95], v[112:115], v[156:159]
buffer_load_dwordx4 v19, s[24:27], s84 offen offset:3072 lds
s_waitcnt lgkmcnt(6)
v_mfma_f32_16x16x32_bf16 v[168:171], v[60:63], v[60:63], v[168:171]
ds_read_b128 v[88:91], v47 offset:24576
v_mfma_f32_16x16x32_bf16 v[160:163], v[60:63], v[56:59], v[160:163]
ds_read_b128 v[92:95], v47 offset:26624
s_waitcnt lgkmcnt(7)
v_mfma_f32_16x16x32_bf16 v[172:175], v[64:67], v[64:67], v[172:175]
ds_read_b128 v[96:99], v40 offset:24576
v_mfma_f32_16x16x32_bf16 v[164:167], v[64:67], v[56:59], v[164:167]
ds_read_b128 v[100:103], v41 offset:24576
s_waitcnt lgkmcnt(8)
v_mfma_f32_16x16x32_bf16 v[176:179], v[56:59], v[68:71], v[176:179]
ds_read_b128 v[104:107], v42 offset:24576
v_mfma_f32_16x16x32_bf16 v[120:123], v[60:63], v[68:71], v[120:123]
ds_read_b128 v[108:111], v43 offset:24576
v_mfma_f32_16x16x32_bf16 v[124:127], v[64:67], v[68:71], v[124:127]
ds_read_b128 v[112:115], v49 offset:6144
s_waitcnt lgkmcnt(10)
v_mfma_f32_16x16x32_bf16 v[128:131], v[60:63], v[72:75], v[128:131]
ds_read_b128 v[116:119], v50 offset:4096
v_mfma_f32_16x16x32_bf16 v[132:135], v[64:67], v[72:75], v[132:135]
s_waitcnt lgkmcnt(10)
v_mfma_f32_16x16x32_bf16 v[136:139], v[60:63], v[76:79], v[136:139]
s_add_u32 m0, s96, 131072
v_mfma_f32_16x16x32_bf16 v[140:143], v[64:67], v[76:79], v[140:143]
buffer_load_dwordx4 v20, s[28:31], s84 offen offset:0 lds
s_waitcnt lgkmcnt(9)
v_mfma_f32_16x16x32_bf16 v[144:147], v[60:63], v[80:83], v[144:147]
v_mfma_f32_16x16x32_bf16 v[148:151], v[64:67], v[80:83], v[148:151]
buffer_load_dwordx4 v21, s[28:31], s84 offen offset:1024 lds
s_waitcnt lgkmcnt(8)
v_mfma_f32_16x16x32_bf16 v[152:155], v[60:63], v[84:87], v[152:155]
s_add_u32 m0, s97, 45056
)A"
R"A(s_cmp_eq_u32 s74, 0
s_cselect_b32 s91, s84, s85
v_mfma_f32_16x16x32_bf16 v[156:159], v[64:67], v[84:87], v[156:159]
buffer_load_dwordx4 v22, s[28:31], s91 offen offset:0 lds
s_waitcnt vmcnt(13) lgkmcnt(0)
s_barrier
v_mfma_f32_16x16x32_bf16 v[180:183], v[56:59], v[116:119], v[180:183]
ds_read_b128 v[60:63], v44 offset:0
v_mfma_f32_16x16x32_bf16 v[168:171], v[88:91], v[88:91], v[168:171]
ds_read_b128 v[64:67], v44 offset:2048
v_mfma_f32_16x16x32_bf16 v[160:163], v[88:91], v[56:59], v[160:163]
ds_read_b128 v[68:71], v28 offset:0
v_mfma_f32_16x16x32_bf16 v[172:175], v[92:95], v[92:95], v[172:175]
ds_read_b128 v[72:75], v29 offset:0
v_mfma_f32_16x16x32_bf16 v[164:167], v[92:95], v[56:59], v[164:167]
ds_read_b128 v[76:79], v30 offset:0
v_mfma_f32_16x16x32_bf16 v[176:179], v[56:59], v[96:99], v[176:179]
ds_read_b128 v[80:83], v31 offset:0
v_mfma_f32_16x16x32_bf16 v[120:123], v[88:91], v[96:99], v[120:123]
ds_read_b128 v[84:87], v48 offset:8192
v_mfma_f32_16x16x32_bf16 v[124:127], v[92:95], v[96:99], v[124:127]
v_mfma_f32_16x16x32_bf16 v[128:131], v[88:91], v[100:103], v[128:131]
s_add_u32 m0, s99, 139264
v_mfma_f32_16x16x32_bf16 v[132:135], v[92:95], v[100:103], v[132:135]
buffer_load_dwordx4 v16, s[24:27], s85 offen offset:0 lds
v_mfma_f32_16x16x32_bf16 v[136:139], v[88:91], v[104:107], v[136:139]
v_mfma_f32_16x16x32_bf16 v[140:143], v[92:95], v[104:107], v[140:143]
buffer_load_dwordx4 v17, s[24:27], s85 offen offset:1024 lds
v_mfma_f32_16x16x32_bf16 v[144:147], v[88:91], v[108:111], v[144:147]
v_mfma_f32_16x16x32_bf16 v[148:151], v[92:95], v[108:111], v[148:151]
buffer_load_dwordx4 v18, s[24:27], s85 offen offset:2048 lds
v_mfma_f32_16x16x32_bf16 v[152:155], v[88:91], v[112:115], v[152:155]
v_mfma_f32_16x16x32_bf16 v[156:159], v[92:95], v[112:115], v[156:159]
buffer_load_dwordx4 v19, s[24:27], s85 offen offset:3072 lds
s_waitcnt lgkmcnt(6)
v_mfma_f32_16x16x32_bf16 v[168:171], v[60:63], v[60:63], v[168:171]
ds_read_b128 v[88:91], v45 offset:0
v_mfma_f32_16x16x32_bf16 v[160:163], v[60:63], v[56:59], v[160:163]
ds_read_b128 v[92:95], v45 offset:2048
s_waitcnt lgkmcnt(7)
v_mfma_f32_16x16x32_bf16 v[172:175], v[64:67], v[64:67], v[172:175]
ds_read_b128 v[96:99], v32 offset:0
v_mfma_f32_16x16x32_bf16 v[164:167], v[64:67], v[56:59], v[164:167]
ds_read_b128 v[100:103], v33 offset:0
s_waitcnt lgkmcnt(8)
v_mfma_f32_16x16x32_bf16 v[176:179], v[56:59], v[68:71], v[176:179]
ds_read_b128 v[104:107], v34 offset:0
v_mfma_f32_16x16x32_bf16 v[120:123], v[60:63], v[68:71], v[120:123]
ds_read_b128 v[108:111], v35 offset:0
v_mfma_f32_16x16x32_bf16 v[124:127], v[64:67], v[68:71], v[124:127]
ds_read_b128 v[112:115], v49 offset:8192
s_waitcnt lgkmcnt(10)
v_mfma_f32_16x16x32_bf16 v[128:131], v[60:63], v[72:75], v[128:131]
v_mfma_f32_16x16x32_bf16 v[132:135], v[64:67], v[72:75], v[132:135]
s_waitcnt lgkmcnt(9)
v_mfma_f32_16x16x32_bf16 v[136:139], v[60:63], v[76:79], v[136:139]
v_mfma_f32_16x16x32_bf16 v[140:143], v[64:67], v[76:79], v[140:143]
s_waitcnt lgkmcnt(8)
v_mfma_f32_16x16x32_bf16 v[144:147], v[60:63], v[80:83], v[144:147]
s_add_u32 m0, s96, 155648
v_mfma_f32_16x16x32_bf16 v[148:151], v[64:67], v[80:83], v[148:151]
buffer_load_dwordx4 v20, s[28:31], s85 offen offset:0 lds
s_waitcnt lgkmcnt(7)
v_mfma_f32_16x16x32_bf16 v[152:155], v[60:63], v[84:87], v[152:155]
v_mfma_f32_16x16x32_bf16 v[156:159], v[64:67], v[84:87], v[156:159]
buffer_load_dwordx4 v21, s[28:31], s85 offen offset:1024 lds
s_waitcnt vmcnt(13) lgkmcnt(0)
s_barrier
v_mfma_f32_16x16x32_bf16 v[168:171], v[88:91], v[88:91], v[168:171]
ds_read_b128 v[60:63], v44 offset:24576
v_mfma_f32_16x16x32_bf16 v[160:163], v[88:91], v[56:59], v[160:163]
ds_read_b128 v[64:67], v44 offset:26624
v_mfma_f32_16x16x32_bf16 v[172:175], v[92:95], v[92:95], v[172:175]
ds_read_b128 v[68:71], v28 offset:24576
v_mfma_f32_16x16x32_bf16 v[164:167], v[92:95], v[56:59], v[164:167]
ds_read_b128 v[72:75], v29 offset:24576
v_mfma_f32_16x16x32_bf16 v[176:179], v[56:59], v[96:99], v[176:179]
ds_read_b128 v[76:79], v30 offset:24576
v_mfma_f32_16x16x32_bf16 v[120:123], v[88:91], v[96:99], v[120:123]
ds_read_b128 v[80:83], v31 offset:24576
v_mfma_f32_16x16x32_bf16 v[124:127], v[92:95], v[96:99], v[124:127]
ds_read_b128 v[84:87], v48 offset:10240
v_mfma_f32_16x16x32_bf16 v[128:131], v[88:91], v[100:103], v[128:131]
s_add_u32 m0, s99, 65536
v_mfma_f32_16x16x32_bf16 v[132:135], v[92:95], v[100:103], v[132:135]
buffer_load_dwordx4 v16, s[24:27], s86 offen offset:0 lds
v_mfma_f32_16x16x32_bf16 v[136:139], v[88:91], v[104:107], v[136:139]
v_mfma_f32_16x16x32_bf16 v[140:143], v[92:95], v[104:107], v[140:143]
buffer_load_dwordx4 v17, s[24:27], s86 offen offset:1024 lds
v_mfma_f32_16x16x32_bf16 v[144:147], v[88:91], v[108:111], v[144:147]
v_mfma_f32_16x16x32_bf16 v[148:151], v[92:95], v[108:111], v[148:151]
buffer_load_dwordx4 v18, s[24:27], s86 offen offset:2048 lds
v_mfma_f32_16x16x32_bf16 v[152:155], v[88:91], v[112:115], v[152:155]
v_mfma_f32_16x16x32_bf16 v[156:159], v[92:95], v[112:115], v[156:159]
buffer_load_dwordx4 v19, s[24:27], s86 offen offset:3072 lds
s_waitcnt lgkmcnt(6)
v_mfma_f32_16x16x32_bf16 v[168:171], v[60:63], v[60:63], v[168:171]
ds_read_b128 v[88:91], v45 offset:24576
v_mfma_f32_16x16x32_bf16 v[160:163], v[60:63], v[56:59], v[160:163]
ds_read_b128 v[92:95], v45 offset:26624
s_waitcnt lgkmcnt(7)
v_mfma_f32_16x16x32_bf16 v[172:175], v[64:67], v[64:67], v[172:175]
ds_read_b128 v[96:99], v32 offset:24576
v_mfma_f32_16x16x32_bf16 v[164:167], v[64:67], v[56:59], v[164:167]
ds_read_b128 v[100:103], v33 offset:24576
s_waitcnt lgkmcnt(8)
v_mfma_f32_16x16x32_bf16 v[176:179], v[56:59], v[68:71], v[176:179]
ds_read_b128 v[104:107], v34 offset:24576
v_mfma_f32_16x16x32_bf16 v[120:123], v[60:63], v[68:71], v[120:123]
ds_read_b128 v[108:111], v35 offset:24576
v_mfma_f32_16x16x32_bf16 v[124:127], v[64:67], v[68:71], v[124:127]
ds_read_b128 v[112:115], v49 offset:10240
s_waitcnt lgkmcnt(10)
v_mfma_f32_16x16x32_bf16 v[128:131], v[60:63], v[72:75], v[128:131]
ds_read_b128 v[116:119], v50 offset:8192
v_mfma_f32_16x16x32_bf16 v[132:135], v[64:67], v[72:75], v[132:135]
s_waitcnt lgkmcnt(10)
v_mfma_f32_16x16x32_bf16 v[136:139], v[60:63], v[76:79], v[136:139]
s_add_u32 m0, s96, 81920
v_mfma_f32_16x16x32_bf16 v[140:143], v[64:67], v[76:79], v[140:143]
buffer_load_dwordx4 v20, s[28:31], s86 offen offset:0 lds
s_waitcnt lgkmcnt(9)
v_mfma_f32_16x16x32_bf16 v[144:147], v[60:63], v[80:83], v[144:147]
v_mfma_f32_16x16x32_bf16 v[148:151], v[64:67], v[80:83], v[148:151]
buffer_load_dwordx4 v21, s[28:31], s86 offen offset:1024 lds
s_waitcnt lgkmcnt(8)
v_mfma_f32_16x16x32_bf16 v[152:155], v[60:63], v[84:87], v[152:155]
s_add_u32 m0, s97, 49152
s_cmp_eq_u32 s74, 0
s_cselect_b32 s91, s86, s87
v_mfma_f32_16x16x32_bf16 v[156:159], v[64:67], v[84:87], v[156:159]
buffer_load_dwordx4 v22, s[28:31], s91 offen offset:0 lds
s_waitcnt vmcnt(13) lgkmcnt(0)
s_barrier
v_mfma_f32_16x16x32_bf16 v[180:183], v[56:59], v[116:119], v[180:183]
ds_read_b128 v[60:63], v46 offset:0
v_mfma_f32_16x16x32_bf16 v[168:171], v[88:91], v[88:91], v[168:171]
ds_read_b128 v[64:67], v46 offset:2048
v_mfma_f32_16x16x32_bf16 v[160:163], v[88:91], v[56:59], v[160:163]
ds_read_b128 v[68:71], v36 offset:0
v_mfma_f32_16x16x32_bf16 v[172:175], v[92:95], v[92:95], v[172:175]
ds_read_b128 v[72:75], v37 offset:0
v_mfma_f32_16x16x32_bf16 v[164:167], v[92:95], v[56:59], v[164:167]
ds_read_b128 v[76:79], v38 offset:0
v_mfma_f32_16x16x32_bf16 v[176:179], v[56:59], v[96:99], v[176:179]
ds_read_b128 v[80:83], v39 offset:0
v_mfma_f32_16x16x32_bf16 v[120:123], v[88:91], v[96:99], v[120:123]
ds_read_b128 v[84:87], v48 offset:12288
v_mfma_f32_16x16x32_bf16 v[124:127], v[92:95], v[96:99], v[124:127]
v_mfma_f32_16x16x32_bf16 v[128:131], v[88:91], v[100:103], v[128:131]
s_add_u32 m0, s99, 90112
v_mfma_f32_16x16x32_bf16 v[132:135], v[92:95], v[100:103], v[132:135]
buffer_load_dwordx4 v16, s[24:27], s87 offen offset:0 lds
v_mfma_f32_16x16x32_bf16 v[136:139], v[88:91], v[104:107], v[136:139]
v_mfma_f32_16x16x32_bf16 v[140:143], v[92:95], v[104:107], v[140:143]
)A"
R"A(buffer_load_dwordx4 v17, s[24:27], s87 offen offset:1024 lds
v_mfma_f32_16x16x32_bf16 v[144:147], v[88:91], v[108:111], v[144:147]
v_mfma_f32_16x16x32_bf16 v[148:151], v[92:95], v[108:111], v[148:151]
buffer_load_dwordx4 v18, s[24:27], s87 offen offset:2048 lds
v_mfma_f32_16x16x32_bf16 v[152:155], v[88:91], v[112:115], v[152:155]
v_mfma_f32_16x16x32_bf16 v[156:159], v[92:95], v[112:115], v[156:159]
buffer_load_dwordx4 v19, s[24:27], s87 offen offset:3072 lds
s_waitcnt lgkmcnt(6)
v_mfma_f32_16x16x32_bf16 v[168:171], v[60:63], v[60:63], v[168:171]
ds_read_b128 v[88:91], v47 offset:0
v_mfma_f32_16x16x32_bf16 v[160:163], v[60:63], v[56:59], v[160:163]
ds_read_b128 v[92:95], v47 offset:2048
s_waitcnt lgkmcnt(7)
v_mfma_f32_16x16x32_bf16 v[172:175], v[64:67], v[64:67], v[172:175]
ds_read_b128 v[96:99], v40 offset:0
v_mfma_f32_16x16x32_bf16 v[164:167], v[64:67], v[56:59], v[164:167]
ds_read_b128 v[100:103], v41 offset:0
s_waitcnt lgkmcnt(8)
v_mfma_f32_16x16x32_bf16 v[176:179], v[56:59], v[68:71], v[176:179]
ds_read_b128 v[104:107], v42 offset:0
v_mfma_f32_16x16x32_bf16 v[120:123], v[60:63], v[68:71], v[120:123]
ds_read_b128 v[108:111], v43 offset:0
v_mfma_f32_16x16x32_bf16 v[124:127], v[64:67], v[68:71], v[124:127]
ds_read_b128 v[112:115], v49 offset:12288
s_waitcnt lgkmcnt(10)
v_mfma_f32_16x16x32_bf16 v[128:131], v[60:63], v[72:75], v[128:131]
s_add_u32 m0, s96, 106496
v_mfma_f32_16x16x32_bf16 v[132:135], v[64:67], v[72:75], v[132:135]
buffer_load_dwordx4 v20, s[28:31], s87 offen offset:0 lds
s_waitcnt lgkmcnt(9)
v_mfma_f32_16x16x32_bf16 v[136:139], v[60:63], v[76:79], v[136:139]
v_mfma_f32_16x16x32_bf16 v[140:143], v[64:67], v[76:79], v[140:143]
buffer_load_dwordx4 v21, s[28:31], s87 offen offset:1024 lds
s_waitcnt lgkmcnt(8)
v_mfma_f32_16x16x32_bf16 v[144:147], v[60:63], v[80:83], v[144:147]
s_add_u32 m0, s94, 0
s_lshl_b32 s91, s19, 1
s_lshl_b32 s91, s91, 7
s_add_u32 s91, s91, s12
v_mfma_f32_16x16x32_bf16 v[148:151], v[64:67], v[80:83], v[148:151]
buffer_load_dwordx4 v24, s[32:35], s91 offen offset:0 lds
s_waitcnt lgkmcnt(7)
v_mfma_f32_16x16x32_bf16 v[152:155], v[60:63], v[84:87], v[152:155]
v_mfma_f32_16x16x32_bf16 v[156:159], v[64:67], v[84:87], v[156:159]
buffer_load_dwordx4 v25, s[32:35], s91 offen offset:1024 lds
s_waitcnt vmcnt(15) lgkmcnt(0)
s_barrier
v_mfma_f32_16x16x32_bf16 v[168:171], v[88:91], v[88:91], v[168:171]
ds_read_b128 v[60:63], v46 offset:24576
v_mfma_f32_16x16x32_bf16 v[160:163], v[88:91], v[56:59], v[160:163]
ds_read_b128 v[64:67], v46 offset:26624
v_mfma_f32_16x16x32_bf16 v[172:175], v[92:95], v[92:95], v[172:175]
ds_read_b128 v[68:71], v36 offset:24576
v_mfma_f32_16x16x32_bf16 v[164:167], v[92:95], v[56:59], v[164:167]
ds_read_b128 v[72:75], v37 offset:24576
v_mfma_f32_16x16x32_bf16 v[176:179], v[56:59], v[96:99], v[176:179]
ds_read_b128 v[76:79], v38 offset:24576
v_mfma_f32_16x16x32_bf16 v[120:123], v[88:91], v[96:99], v[120:123]
ds_read_b128 v[80:83], v39 offset:24576
v_mfma_f32_16x16x32_bf16 v[124:127], v[92:95], v[96:99], v[124:127]
ds_read_b128 v[84:87], v48 offset:14336
v_mfma_f32_16x16x32_bf16 v[128:131], v[88:91], v[100:103], v[128:131]
v_mfma_f32_16x16x32_bf16 v[132:135], v[92:95], v[100:103], v[132:135]
buffer_load_dwordx4 v26, s[32:35], s91 offen offset:2048 lds
v_mfma_f32_16x16x32_bf16 v[136:139], v[88:91], v[104:107], v[136:139]
v_mfma_f32_16x16x32_bf16 v[140:143], v[92:95], v[104:107], v[140:143]
buffer_load_dwordx4 v27, s[32:35], s91 offen offset:3072 lds
v_mfma_f32_16x16x32_bf16 v[144:147], v[88:91], v[108:111], v[144:147]
s_add_u32 m0, s94, 4096
s_add_u32 s91, s91, 131072
v_mfma_f32_16x16x32_bf16 v[148:151], v[92:95], v[108:111], v[148:151]
buffer_load_dwordx4 v24, s[32:35], s91 offen offset:0 lds
v_mfma_f32_16x16x32_bf16 v[152:155], v[88:91], v[112:115], v[152:155]
v_mfma_f32_16x16x32_bf16 v[156:159], v[92:95], v[112:115], v[156:159]
buffer_load_dwordx4 v25, s[32:35], s91 offen offset:1024 lds
s_waitcnt lgkmcnt(6)
v_mfma_f32_16x16x32_bf16 v[168:171], v[60:63], v[60:63], v[168:171]
ds_read_b128 v[88:91], v47 offset:24576
v_mfma_f32_16x16x32_bf16 v[160:163], v[60:63], v[56:59], v[160:163]
ds_read_b128 v[92:95], v47 offset:26624
s_waitcnt lgkmcnt(7)
v_mfma_f32_16x16x32_bf16 v[172:175], v[64:67], v[64:67], v[172:175]
ds_read_b128 v[96:99], v40 offset:24576
v_mfma_f32_16x16x32_bf16 v[164:167], v[64:67], v[56:59], v[164:167]
ds_read_b128 v[100:103], v41 offset:24576
s_waitcnt lgkmcnt(8)
v_mfma_f32_16x16x32_bf16 v[176:179], v[56:59], v[68:71], v[176:179]
ds_read_b128 v[104:107], v42 offset:24576
v_mfma_f32_16x16x32_bf16 v[120:123], v[60:63], v[68:71], v[120:123]
ds_read_b128 v[108:111], v43 offset:24576
v_mfma_f32_16x16x32_bf16 v[124:127], v[64:67], v[68:71], v[124:127]
ds_read_b128 v[112:115], v49 offset:14336
s_waitcnt lgkmcnt(10)
v_mfma_f32_16x16x32_bf16 v[128:131], v[60:63], v[72:75], v[128:131]
ds_read_b128 v[116:119], v50 offset:12288
v_mfma_f32_16x16x32_bf16 v[132:135], v[64:67], v[72:75], v[132:135]
buffer_load_dwordx4 v26, s[32:35], s91 offen offset:2048 lds
s_waitcnt lgkmcnt(10)
v_mfma_f32_16x16x32_bf16 v[136:139], v[60:63], v[76:79], v[136:139]
v_mfma_f32_16x16x32_bf16 v[140:143], v[64:67], v[76:79], v[140:143]
buffer_load_dwordx4 v27, s[32:35], s91 offen offset:3072 lds
s_waitcnt lgkmcnt(9)
v_mfma_f32_16x16x32_bf16 v[144:147], v[60:63], v[80:83], v[144:147]
s_add_u32 m0, s99, 114688
v_mfma_f32_16x16x32_bf16 v[148:151], v[64:67], v[80:83], v[148:151]
buffer_load_dwordx4 v207, s[36:39], s40 offen offset:0 lds
s_waitcnt lgkmcnt(8)
v_mfma_f32_16x16x32_bf16 v[152:155], v[60:63], v[84:87], v[152:155]
v_mfma_f32_16x16x32_bf16 v[156:159], v[64:67], v[84:87], v[156:159]
buffer_load_dwordx4 v207, s[36:39], s41 offen offset:1024 lds
s_waitcnt vmcnt(16) lgkmcnt(0)
s_barrier
v_mfma_f32_16x16x32_bf16 v[180:183], v[56:59], v[116:119], v[180:183]
ds_read_b128 v[60:63], v44 offset:0
v_mfma_f32_16x16x32_bf16 v[168:171], v[88:91], v[88:91], v[168:171]
ds_read_b128 v[64:67], v44 offset:2048
v_mfma_f32_16x16x32_bf16 v[160:163], v[88:91], v[56:59], v[160:163]
ds_read_b128 v[68:71], v28 offset:0
v_mfma_f32_16x16x32_bf16 v[172:175], v[92:95], v[92:95], v[172:175]
ds_read_b128 v[72:75], v29 offset:0
v_mfma_f32_16x16x32_bf16 v[164:167], v[92:95], v[56:59], v[164:167]
ds_read_b128 v[76:79], v30 offset:0
v_mfma_f32_16x16x32_bf16 v[176:179], v[56:59], v[96:99], v[176:179]
ds_read_b128 v[80:83], v31 offset:0
v_mfma_f32_16x16x32_bf16 v[120:123], v[88:91], v[96:99], v[120:123]
ds_read_b128 v[84:87], v48 offset:16384
v_mfma_f32_16x16x32_bf16 v[124:127], v[92:95], v[96:99], v[124:127]
v_mfma_f32_16x16x32_bf16 v[128:131], v[88:91], v[100:103], v[128:131]
v_mfma_f32_16x16x32_bf16 v[132:135], v[92:95], v[100:103], v[132:135]
v_mfma_f32_16x16x32_bf16 v[136:139], v[88:91], v[104:107], v[136:139]
v_mfma_f32_16x16x32_bf16 v[140:143], v[92:95], v[104:107], v[140:143]
v_mfma_f32_16x16x32_bf16 v[144:147], v[88:91], v[108:111], v[144:147]
v_mfma_f32_16x16x32_bf16 v[148:151], v[92:95], v[108:111], v[148:151]
buffer_load_dwordx4 v207, s[36:39], s42 offen offset:2048 lds
v_mfma_f32_16x16x32_bf16 v[152:155], v[88:91], v[112:115], v[152:155]
v_mfma_f32_16x16x32_bf16 v[156:159], v[92:95], v[112:115], v[156:159]
buffer_load_dwordx4 v207, s[36:39], s43 offen offset:3072 lds
s_waitcnt lgkmcnt(6)
v_mfma_f32_16x16x32_bf16 v[168:171], v[60:63], v[60:63], v[168:171]
ds_read_b128 v[88:91], v45 offset:0
v_mfma_f32_16x16x32_bf16 v[160:163], v[60:63], v[56:59], v[160:163]
ds_read_b128 v[92:95], v45 offset:2048
s_waitcnt lgkmcnt(7)
v_mfma_f32_16x16x32_bf16 v[172:175], v[64:67], v[64:67], v[172:175]
ds_read_b128 v[96:99], v32 offset:0
v_mfma_f32_16x16x32_bf16 v[164:167], v[64:67], v[56:59], v[164:167]
ds_read_b128 v[100:103], v33 offset:0
s_waitcnt lgkmcnt(8)
v_mfma_f32_16x16x32_bf16 v[176:179], v[56:59], v[68:71], v[176:179]
ds_read_b128 v[104:107], v34 offset:0
v_mfma_f32_16x16x32_bf16 v[120:123], v[60:63], v[68:71], v[120:123]
ds_read_b128 v[108:111], v35 offset:0
v_mfma_f32_16x16x32_bf16 v[124:127], v[64:67], v[68:71], v[124:127]
ds_read_b128 v[112:115], v49 offset:16384
)A"
R"A(s_waitcnt lgkmcnt(10)
v_mfma_f32_16x16x32_bf16 v[128:131], v[60:63], v[72:75], v[128:131]
v_mfma_f32_16x16x32_bf16 v[132:135], v[64:67], v[72:75], v[132:135]
s_waitcnt lgkmcnt(9)
v_mfma_f32_16x16x32_bf16 v[136:139], v[60:63], v[76:79], v[136:139]
v_mfma_f32_16x16x32_bf16 v[140:143], v[64:67], v[76:79], v[140:143]
s_waitcnt lgkmcnt(8)
v_mfma_f32_16x16x32_bf16 v[144:147], v[60:63], v[80:83], v[144:147]
v_mfma_f32_16x16x32_bf16 v[148:151], v[64:67], v[80:83], v[148:151]
s_waitcnt lgkmcnt(7)
v_mfma_f32_16x16x32_bf16 v[152:155], v[60:63], v[84:87], v[152:155]
v_mfma_f32_16x16x32_bf16 v[156:159], v[64:67], v[84:87], v[156:159]
s_waitcnt vmcnt(12) lgkmcnt(0)
s_barrier
v_mfma_f32_16x16x32_bf16 v[168:171], v[88:91], v[88:91], v[168:171]
ds_read_b128 v[60:63], v44 offset:24576
v_mfma_f32_16x16x32_bf16 v[160:163], v[88:91], v[56:59], v[160:163]
ds_read_b128 v[64:67], v44 offset:26624
v_mfma_f32_16x16x32_bf16 v[172:175], v[92:95], v[92:95], v[172:175]
ds_read_b128 v[68:71], v28 offset:24576
v_mfma_f32_16x16x32_bf16 v[164:167], v[92:95], v[56:59], v[164:167]
ds_read_b128 v[72:75], v29 offset:24576
v_mfma_f32_16x16x32_bf16 v[176:179], v[56:59], v[96:99], v[176:179]
ds_read_b128 v[76:79], v30 offset:24576
v_mfma_f32_16x16x32_bf16 v[120:123], v[88:91], v[96:99], v[120:123]
ds_read_b128 v[80:83], v31 offset:24576
v_mfma_f32_16x16x32_bf16 v[124:127], v[92:95], v[96:99], v[124:127]
ds_read_b128 v[84:87], v48 offset:18432
v_mfma_f32_16x16x32_bf16 v[128:131], v[88:91], v[100:103], v[128:131]
v_mfma_f32_16x16x32_bf16 v[132:135], v[92:95], v[100:103], v[132:135]
v_mfma_f32_16x16x32_bf16 v[136:139], v[88:91], v[104:107], v[136:139]
v_mfma_f32_16x16x32_bf16 v[140:143], v[92:95], v[104:107], v[140:143]
v_mfma_f32_16x16x32_bf16 v[144:147], v[88:91], v[108:111], v[144:147]
v_mfma_f32_16x16x32_bf16 v[148:151], v[92:95], v[108:111], v[148:151]
v_mfma_f32_16x16x32_bf16 v[152:155], v[88:91], v[112:115], v[152:155]
v_mfma_f32_16x16x32_bf16 v[156:159], v[92:95], v[112:115], v[156:159]
s_waitcnt lgkmcnt(6)
v_mfma_f32_16x16x32_bf16 v[168:171], v[60:63], v[60:63], v[168:171]
ds_read_b128 v[88:91], v45 offset:24576
v_mfma_f32_16x16x32_bf16 v[160:163], v[60:63], v[56:59], v[160:163]
ds_read_b128 v[92:95], v45 offset:26624
s_waitcnt lgkmcnt(7)
v_mfma_f32_16x16x32_bf16 v[172:175], v[64:67], v[64:67], v[172:175]
ds_read_b128 v[96:99], v32 offset:24576
v_mfma_f32_16x16x32_bf16 v[164:167], v[64:67], v[56:59], v[164:167]
ds_read_b128 v[100:103], v33 offset:24576
s_waitcnt lgkmcnt(8)
v_mfma_f32_16x16x32_bf16 v[176:179], v[56:59], v[68:71], v[176:179]
ds_read_b128 v[104:107], v34 offset:24576
v_mfma_f32_16x16x32_bf16 v[120:123], v[60:63], v[68:71], v[120:123]
ds_read_b128 v[108:111], v35 offset:24576
v_mfma_f32_16x16x32_bf16 v[124:127], v[64:67], v[68:71], v[124:127]
ds_read_b128 v[112:115], v49 offset:18432
s_waitcnt lgkmcnt(10)
v_mfma_f32_16x16x32_bf16 v[128:131], v[60:63], v[72:75], v[128:131]
ds_read_b128 v[116:119], v50 offset:16384
v_mfma_f32_16x16x32_bf16 v[132:135], v[64:67], v[72:75], v[132:135]
s_waitcnt lgkmcnt(10)
v_mfma_f32_16x16x32_bf16 v[136:139], v[60:63], v[76:79], v[136:139]
v_mfma_f32_16x16x32_bf16 v[140:143], v[64:67], v[76:79], v[140:143]
s_waitcnt lgkmcnt(9)
v_mfma_f32_16x16x32_bf16 v[144:147], v[60:63], v[80:83], v[144:147]
v_mfma_f32_16x16x32_bf16 v[148:151], v[64:67], v[80:83], v[148:151]
s_waitcnt lgkmcnt(8)
v_mfma_f32_16x16x32_bf16 v[152:155], v[60:63], v[84:87], v[152:155]
v_mfma_f32_16x16x32_bf16 v[156:159], v[64:67], v[84:87], v[156:159]
Lp1e_%=:
s_waitcnt lgkmcnt(0)
v_mfma_f32_16x16x32_bf16 v[180:183], v[56:59], v[116:119], v[180:183]
v_mfma_f32_16x16x32_bf16 v[168:171], v[88:91], v[88:91], v[168:171]
v_mfma_f32_16x16x32_bf16 v[160:163], v[88:91], v[56:59], v[160:163]
v_mfma_f32_16x16x32_bf16 v[172:175], v[92:95], v[92:95], v[172:175]
v_mfma_f32_16x16x32_bf16 v[164:167], v[92:95], v[56:59], v[164:167]
v_mfma_f32_16x16x32_bf16 v[176:179], v[56:59], v[96:99], v[176:179]
v_mfma_f32_16x16x32_bf16 v[120:123], v[88:91], v[96:99], v[120:123]
v_mfma_f32_16x16x32_bf16 v[124:127], v[92:95], v[96:99], v[124:127]
v_mfma_f32_16x16x32_bf16 v[128:131], v[88:91], v[100:103], v[128:131]
s_nop 0
ds_bpermute_b32 v208, v247, v168 offset:0
ds_bpermute_b32 v209, v247, v169 offset:4
v_mfma_f32_16x16x32_bf16 v[132:135], v[92:95], v[100:103], v[132:135]
ds_bpermute_b32 v210, v247, v170 offset:8
ds_bpermute_b32 v211, v247, v171 offset:12
v_mfma_f32_16x16x32_bf16 v[136:139], v[88:91], v[104:107], v[136:139]
ds_bpermute_b32 v212, v247, v172 offset:0
ds_bpermute_b32 v213, v247, v173 offset:4
v_mfma_f32_16x16x32_bf16 v[140:143], v[92:95], v[104:107], v[140:143]
ds_bpermute_b32 v214, v247, v174 offset:8
ds_bpermute_b32 v215, v247, v175 offset:12
v_mfma_f32_16x16x32_bf16 v[144:147], v[88:91], v[108:111], v[144:147]
ds_write_b32 v249, v180 offset:62464
ds_write_b32 v248, v176 offset:61440
v_mfma_f32_16x16x32_bf16 v[148:151], v[92:95], v[108:111], v[148:151]
v_mul_f32_e32 v216, v10, v160
v_mul_f32_e32 v217, v10, v161
v_mul_f32_e32 v218, v10, v162
v_mfma_f32_16x16x32_bf16 v[152:155], v[88:91], v[112:115], v[152:155]
v_mul_f32_e32 v219, v10, v163
v_mul_f32_e32 v220, v10, v164
v_mul_f32_e32 v221, v10, v165
v_mfma_f32_16x16x32_bf16 v[156:159], v[92:95], v[112:115], v[156:159]
v_mul_f32_e32 v222, v10, v166
v_mul_f32_e32 v223, v10, v167
s_waitcnt lgkmcnt(0)
s_barrier
ds_read_b128 v[232:235], v250 offset:61440
ds_read_b128 v[236:239], v251 offset:62464
v_pk_fma_f32 v[224:225], v[216:217], v[216:217], v[12:13] op_sel_hi:[1,1,0] neg_lo:[1,0,0] neg_hi:[1,0,0]
v_pk_fma_f32 v[226:227], v[218:219], v[218:219], v[12:13] op_sel_hi:[1,1,0] neg_lo:[1,0,0] neg_hi:[1,0,0]
v_pk_fma_f32 v[228:229], v[220:221], v[220:221], v[12:13] op_sel_hi:[1,1,0] neg_lo:[1,0,0] neg_hi:[1,0,0]
v_pk_fma_f32 v[230:231], v[222:223], v[222:223], v[12:13] op_sel_hi:[1,1,0] neg_lo:[1,0,0] neg_hi:[1,0,0]
v_pk_fma_f32 v[224:225], v[208:209], v[10:11], v[224:225] op_sel_hi:[1,0,1]
v_pk_fma_f32 v[226:227], v[210:211], v[10:11], v[226:227] op_sel_hi:[1,0,1]
v_pk_fma_f32 v[228:229], v[212:213], v[10:11], v[228:229] op_sel_hi:[1,0,1]
v_pk_fma_f32 v[230:231], v[214:215], v[10:11], v[230:231] op_sel_hi:[1,0,1]
v_max_f32_e32 v224, v224, v12
v_max_f32_e32 v225, v225, v12
v_max_f32_e32 v226, v226, v12
v_max_f32_e32 v227, v227, v12
v_max_f32_e32 v228, v228, v12
v_max_f32_e32 v229, v229, v12
v_max_f32_e32 v230, v230, v12
v_max_f32_e32 v231, v231, v12
v_rsq_f32_e32 v224, v224
v_rsq_f32_e32 v225, v225
v_rsq_f32_e32 v226, v226
v_rsq_f32_e32 v227, v227
v_rsq_f32_e32 v228, v228
v_rsq_f32_e32 v229, v229
v_rsq_f32_e32 v230, v230
v_rsq_f32_e32 v231, v231
s_waitcnt lgkmcnt(0)
v_pk_add_f32 v[236:237], v[236:237], v[238:239]
v_add_f32_e32 v236, v236, v237
v_pk_fma_f32 v[120:121], v[216:217], v[232:233], v[120:121] op_sel:[0,0,0] op_sel_hi:[1,0,1] neg_lo:[1,0,0] neg_hi:[1,0,0]
v_pk_fma_f32 v[122:123], v[218:219], v[232:233], v[122:123] op_sel:[0,0,0] op_sel_hi:[1,0,1] neg_lo:[1,0,0] neg_hi:[1,0,0]
v_pk_fma_f32 v[124:125], v[220:221], v[232:233], v[124:125] op_sel:[0,0,0] op_sel_hi:[1,0,1] neg_lo:[1,0,0] neg_hi:[1,0,0]
v_pk_fma_f32 v[126:127], v[222:223], v[232:233], v[126:127] op_sel:[0,0,0] op_sel_hi:[1,0,1] neg_lo:[1,0,0] neg_hi:[1,0,0]
v_pk_mul_f32 v[120:121], v[120:121], v[224:225]
v_pk_mul_f32 v[122:123], v[122:123], v[226:227]
v_pk_mul_f32 v[124:125], v[124:125], v[228:229]
v_pk_mul_f32 v[126:127], v[126:127], v[230:231]
v_cvt_pk_bf16_f32 v120, v120, v121
v_cvt_pk_bf16_f32 v121, v122, v123
v_cvt_pk_bf16_f32 v122, v124, v125
v_cvt_pk_bf16_f32 v123, v126, v127
buffer_store_dwordx4 v[120:123], v1, s[20:23], s48 offen offset:0 sc1
v_pk_fma_f32 v[128:129], v[216:217], v[232:233], v[128:129] op_sel:[0,1,0] op_sel_hi:[1,1,1] neg_lo:[1,0,0] neg_hi:[1,0,0]
v_pk_fma_f32 v[130:131], v[218:219], v[232:233], v[130:131] op_sel:[0,1,0] op_sel_hi:[1,1,1] neg_lo:[1,0,0] neg_hi:[1,0,0]
v_pk_fma_f32 v[132:133], v[220:221], v[232:233], v[132:133] op_sel:[0,1,0] op_sel_hi:[1,1,1] neg_lo:[1,0,0] neg_hi:[1,0,0]
v_pk_fma_f32 v[134:135], v[222:223], v[232:233], v[134:135] op_sel:[0,1,0] op_sel_hi:[1,1,1] neg_lo:[1,0,0] neg_hi:[1,0,0]
)A"
R"A(v_pk_mul_f32 v[128:129], v[128:129], v[224:225]
v_pk_mul_f32 v[130:131], v[130:131], v[226:227]
v_pk_mul_f32 v[132:133], v[132:133], v[228:229]
v_pk_mul_f32 v[134:135], v[134:135], v[230:231]
v_cvt_pk_bf16_f32 v128, v128, v129
v_cvt_pk_bf16_f32 v129, v130, v131
v_cvt_pk_bf16_f32 v130, v132, v133
v_cvt_pk_bf16_f32 v131, v134, v135
buffer_store_dwordx4 v[128:131], v1, s[20:23], s49 offen offset:0 sc1
v_pk_fma_f32 v[136:137], v[216:217], v[234:235], v[136:137] op_sel:[0,0,0] op_sel_hi:[1,0,1] neg_lo:[1,0,0] neg_hi:[1,0,0]
v_pk_fma_f32 v[138:139], v[218:219], v[234:235], v[138:139] op_sel:[0,0,0] op_sel_hi:[1,0,1] neg_lo:[1,0,0] neg_hi:[1,0,0]
v_pk_fma_f32 v[140:141], v[220:221], v[234:235], v[140:141] op_sel:[0,0,0] op_sel_hi:[1,0,1] neg_lo:[1,0,0] neg_hi:[1,0,0]
v_pk_fma_f32 v[142:143], v[222:223], v[234:235], v[142:143] op_sel:[0,0,0] op_sel_hi:[1,0,1] neg_lo:[1,0,0] neg_hi:[1,0,0]
v_pk_mul_f32 v[136:137], v[136:137], v[224:225]
v_pk_mul_f32 v[138:139], v[138:139], v[226:227]
v_pk_mul_f32 v[140:141], v[140:141], v[228:229]
v_pk_mul_f32 v[142:143], v[142:143], v[230:231]
v_cvt_pk_bf16_f32 v136, v136, v137
v_cvt_pk_bf16_f32 v137, v138, v139
v_cvt_pk_bf16_f32 v138, v140, v141
v_cvt_pk_bf16_f32 v139, v142, v143
buffer_store_dwordx4 v[136:139], v1, s[20:23], s50 offen offset:0 sc1
v_pk_fma_f32 v[144:145], v[216:217], v[234:235], v[144:145] op_sel:[0,1,0] op_sel_hi:[1,1,1] neg_lo:[1,0,0] neg_hi:[1,0,0]
v_pk_fma_f32 v[146:147], v[218:219], v[234:235], v[146:147] op_sel:[0,1,0] op_sel_hi:[1,1,1] neg_lo:[1,0,0] neg_hi:[1,0,0]
v_pk_fma_f32 v[148:149], v[220:221], v[234:235], v[148:149] op_sel:[0,1,0] op_sel_hi:[1,1,1] neg_lo:[1,0,0] neg_hi:[1,0,0]
v_pk_fma_f32 v[150:151], v[222:223], v[234:235], v[150:151] op_sel:[0,1,0] op_sel_hi:[1,1,1] neg_lo:[1,0,0] neg_hi:[1,0,0]
v_pk_mul_f32 v[144:145], v[144:145], v[224:225]
v_pk_mul_f32 v[146:147], v[146:147], v[226:227]
v_pk_mul_f32 v[148:149], v[148:149], v[228:229]
v_pk_mul_f32 v[150:151], v[150:151], v[230:231]
v_cvt_pk_bf16_f32 v144, v144, v145
v_cvt_pk_bf16_f32 v145, v146, v147
v_cvt_pk_bf16_f32 v146, v148, v149
v_cvt_pk_bf16_f32 v147, v150, v151
buffer_store_dwordx4 v[144:147], v1, s[20:23], s51 offen offset:0 sc1
v_pk_fma_f32 v[152:153], v[216:217], v[236:237], v[152:153] op_sel:[0,0,0] op_sel_hi:[1,0,1] neg_lo:[1,0,0] neg_hi:[1,0,0]
v_pk_fma_f32 v[154:155], v[218:219], v[236:237], v[154:155] op_sel:[0,0,0] op_sel_hi:[1,0,1] neg_lo:[1,0,0] neg_hi:[1,0,0]
v_pk_fma_f32 v[156:157], v[220:221], v[236:237], v[156:157] op_sel:[0,0,0] op_sel_hi:[1,0,1] neg_lo:[1,0,0] neg_hi:[1,0,0]
v_pk_fma_f32 v[158:159], v[222:223], v[236:237], v[158:159] op_sel:[0,0,0] op_sel_hi:[1,0,1] neg_lo:[1,0,0] neg_hi:[1,0,0]
v_pk_mul_f32 v[152:153], v[152:153], v[224:225]
v_pk_mul_f32 v[154:155], v[154:155], v[226:227]
v_pk_mul_f32 v[156:157], v[156:157], v[228:229]
v_pk_mul_f32 v[158:159], v[158:159], v[230:231]
v_cvt_pk_bf16_f32 v152, v152, v153
v_cvt_pk_bf16_f32 v153, v154, v155
v_cvt_pk_bf16_f32 v154, v156, v157
v_cvt_pk_bf16_f32 v155, v158, v159
buffer_store_dwordx4 v[152:155], v1, s[20:23], s52 offen offset:0 sc1
s_lshr_b32 s90, s19, 1
s_add_u32 s90, s90, 5
s_lshl_b32 s93, s19, 1
s_add_u32 s93, s93, 1
s_mov_b32 s55, 0
s_mov_b32 s9, 0
s_add_u32 s88, s19, 0
s_and_b32 s88, s88, 7
s_lshl_b32 s88, s88, 1
s_or_b32 s55, s55, s88
s_add_u32 s88, s19, 1
s_and_b32 s88, s88, 7
s_lshl_b32 s88, s88, 5
s_or_b32 s55, s55, s88
s_add_u32 s88, s19, 2
s_and_b32 s88, s88, 7
s_lshl_b32 s88, s88, 9
s_or_b32 s55, s55, s88
s_add_u32 s88, s19, 3
s_and_b32 s88, s88, 7
s_lshl_b32 s88, s88, 13
s_or_b32 s55, s55, s88
s_add_u32 s88, s19, 4
s_and_b32 s88, s88, 7
s_lshl_b32 s88, s88, 17
s_or_b32 s55, s55, s88
s_cmp_lt_u32 s90, 5
s_subb_u32 s88, s19, 3
s_and_b32 s88, s88, 7
s_lshl_b32 s88, s88, 1
s_cmp_eq_u32 s90, 5
s_cselect_b32 s88, s93, s88
s_lshl_b32 s88, s88, 20
s_or_b32 s55, s55, s88
s_cmp_lt_u32 s90, 6
s_subb_u32 s88, s19, 2
s_and_b32 s88, s88, 7
s_lshl_b32 s88, s88, 1
s_cmp_eq_u32 s90, 6
s_cselect_b32 s88, s93, s88
s_lshl_b32 s88, s88, 24
s_or_b32 s55, s55, s88
s_cmp_lt_u32 s90, 7
s_subb_u32 s88, s19, 1
s_and_b32 s88, s88, 7
s_lshl_b32 s88, s88, 1
s_cmp_eq_u32 s90, 7
s_cselect_b32 s88, s93, s88
s_lshl_b32 s88, s88, 28
s_or_b32 s55, s55, s88
s_cmp_lt_u32 s90, 8
s_subb_u32 s88, s19, 0
s_and_b32 s88, s88, 7
s_lshl_b32 s88, s88, 1
s_cmp_eq_u32 s90, 8
s_cselect_b32 s88, s93, s88
s_lshl_b32 s88, s88, 0
s_or_b32 s9, s9, s88
s_cmp_le_u32 s19, 0
s_cselect_b32 s88, 3, 1
s_lshl_b32 s88, s88, 4
s_or_b32 s9, s9, s88
s_cmp_le_u32 s19, 1
s_cselect_b32 s88, 5, 3
s_lshl_b32 s88, s88, 8
s_or_b32 s9, s9, s88
s_cmp_le_u32 s19, 2
s_cselect_b32 s88, 7, 5
s_lshl_b32 s88, s88, 12
s_or_b32 s9, s9, s88
s_cmp_le_u32 s19, 3
s_cselect_b32 s88, 9, 7
s_lshl_b32 s88, s88, 16
s_or_b32 s9, s9, s88
s_cmp_le_u32 s19, 4
s_cselect_b32 s88, 11, 9
s_lshl_b32 s88, s88, 20
s_or_b32 s9, s9, s88
s_cmp_le_u32 s19, 5
s_cselect_b32 s88, 13, 11
s_lshl_b32 s88, s88, 24
s_or_b32 s9, s9, s88
s_cmp_le_u32 s19, 6
s_cselect_b32 s88, 15, 13
s_lshl_b32 s88, s88, 28
s_or_b32 s9, s9, s88
s_bfe_u32 s88, s17, 0x20001
s_lshl_b32 s88, s88, 3
s_lshr_b32 s90, s77, 2
s_add_u32 s76, s88, s90
s_and_b32 s25, s90, 0
s_and_b32 s88, s17, 1
s_lshl_b32 s88, s88, 2
s_and_b32 s90, s77, 3
s_add_u32 s24, s88, s90
s_mul_i32 s88, s76, 196608
s_add_u32 s88, s88, 10616832
s_mul_i32 s85, s16, 24576
s_add_u32 s85, s85, s88
s_mul_i32 s89, s24, 12288
s_add_u32 s88, s88, s89
s_mul_i32 s89, s16, 3072
s_add_u32 s28, s88, s89
s_add_u32 s29, s28, 98304
s_waitcnt vmcnt(5)
ds_read_b64_tr_b16 a[0:1], v54 offset:0
ds_read_b64_tr_b16 a[2:3], v54 offset:2048
ds_read_b64_tr_b16 a[4:5], v55 offset:0
ds_read_b64_tr_b16 a[6:7], v55 offset:2048
ds_read_b64_tr_b16 a[8:9], v54 offset:64
ds_read_b64_tr_b16 a[10:11], v54 offset:2112
ds_read_b64_tr_b16 a[12:13], v55 offset:64
ds_read_b64_tr_b16 a[14:15], v55 offset:2112
s_waitcnt lgkmcnt(6)
buffer_store_dwordx4 a[0:3], v1, s[20:23], s44 offen offset:0 sc1
s_waitcnt lgkmcnt(4)
buffer_store_dwordx4 a[4:7], v1, s[20:23], s45 offen offset:0 sc1
s_waitcnt lgkmcnt(2)
buffer_store_dwordx4 a[8:11], v1, s[20:23], s46 offen offset:0 sc1
s_waitcnt lgkmcnt(0)
buffer_store_dwordx4 a[12:15], v1, s[20:23], s47 offen offset:0 sc1
s_sub_u32 s88, s18, 1
s_and_b32 s90, s88, s98
v_lshlrev_b32_e32 v64, 16, v184
v_and_b32_e32 v65, s90, v184
v_and_b32_e32 v64, s88, v64
v_lshlrev_b32_e32 v66, 16, v185
v_and_b32_e32 v67, s90, v185
v_and_b32_e32 v66, s88, v66
v_lshlrev_b32_e32 v96, 16, v186
v_and_b32_e32 v97, s90, v186
v_and_b32_e32 v96, s88, v96
v_lshlrev_b32_e32 v98, 16, v187
v_and_b32_e32 v99, s90, v187
v_and_b32_e32 v98, s88, v98
v_lshlrev_b32_e32 v68, 16, v188
v_and_b32_e32 v69, s90, v188
v_and_b32_e32 v68, s88, v68
v_lshlrev_b32_e32 v70, 16, v189
v_and_b32_e32 v71, s90, v189
v_and_b32_e32 v70, s88, v70
v_lshlrev_b32_e32 v100, 16, v190
v_and_b32_e32 v101, s90, v190
v_and_b32_e32 v100, s88, v100
v_lshlrev_b32_e32 v102, 16, v191
v_and_b32_e32 v103, s90, v191
v_and_b32_e32 v102, s88, v102
v_lshlrev_b32_e32 v72, 16, v192
v_and_b32_e32 v73, s90, v192
v_and_b32_e32 v72, s88, v72
v_lshlrev_b32_e32 v74, 16, v193
v_and_b32_e32 v75, s90, v193
v_and_b32_e32 v74, s88, v74
v_lshlrev_b32_e32 v104, 16, v194
v_and_b32_e32 v105, s90, v194
v_and_b32_e32 v104, s88, v104
v_lshlrev_b32_e32 v106, 16, v195
v_and_b32_e32 v107, s90, v195
v_and_b32_e32 v106, s88, v106
v_lshlrev_b32_e32 v76, 16, v196
v_and_b32_e32 v77, s90, v196
v_and_b32_e32 v76, s88, v76
v_lshlrev_b32_e32 v78, 16, v197
v_and_b32_e32 v79, s90, v197
v_and_b32_e32 v78, s88, v78
v_lshlrev_b32_e32 v108, 16, v198
v_and_b32_e32 v109, s90, v198
v_and_b32_e32 v108, s88, v108
v_lshlrev_b32_e32 v110, 16, v199
v_and_b32_e32 v111, s90, v199
v_and_b32_e32 v110, s88, v110
v_lshlrev_b32_e32 v128, 16, v200
v_and_b32_e32 v129, s90, v200
v_and_b32_e32 v128, s88, v128
v_lshlrev_b32_e32 v130, 16, v201
v_and_b32_e32 v131, s90, v201
v_and_b32_e32 v130, s88, v130
v_lshlrev_b32_e32 v132, 16, v202
v_and_b32_e32 v133, s90, v202
v_and_b32_e32 v132, s88, v132
v_lshlrev_b32_e32 v134, 16, v203
v_and_b32_e32 v135, s90, v203
)A"
R"A(v_and_b32_e32 v134, s88, v134
s_lshl_b32 s88, s75, 6
v_xor_b32_e32 v6, s88, v15
s_lshl_b32 s88, s74, 14
s_lshl_b32 s90, s75, 12
s_add_u32 s88, s88, s90
v_add_u32_e32 v28, s88, v6
v_xor_b32_e32 v29, 4096, v28
v_xor_b32_e32 v30, 64, v28
v_xor_b32_e32 v31, 64, v29
v_add_u32_e32 v32, 65536, v28
v_add_u32_e32 v33, 65536, v29
v_add_u32_e32 v34, 65536, v30
v_add_u32_e32 v35, 65536, v31
v_add_u32_e32 v36, 65536, v32
v_add_u32_e32 v37, 65536, v33
v_add_u32_e32 v38, 65536, v34
v_add_u32_e32 v39, 65536, v35
s_mul_i32 s88, s75, 7168
v_add_u32_e32 v16, s88, v1
s_mul_i32 s88, s75, 5120
s_add_u32 s88, s88, 4096
v_add_u32_e32 v17, s88, v1
s_mul_i32 s88, s75, 3072
s_sub_u32 s88, 5120, s88
v_add_u32_e32 v18, s88, v1
s_mul_i32 s96, s74, 163840
s_add_u32 s96, s96, s95
s_waitcnt vmcnt(4)
s_barrier
s_cmp_lg_u32 s16, 0
s_cbranch_scc1 Lpb1_%=
v_mov_b32_e32 v8, s70
v_mov_b32_e32 v9, s71
s_mov_b64 exec, 1
buffer_store_dwordx2 v[8:9], off, s[20:23], s54 sc1
s_mov_b64 exec, -1
Lpb1_%=:
s_add_u32 m0, s94, 131072
s_add_u32 s91, s19, 1
s_and_b32 s91, s91, 7
s_lshl_b32 s91, s91, 1
s_lshl_b32 s91, s91, 7
s_add_u32 s91, s91, s12
buffer_load_dwordx4 v24, s[32:35], s91 offen offset:0 lds
buffer_load_dwordx4 v25, s[32:35], s91 offen offset:1024 lds
buffer_load_dwordx4 v26, s[32:35], s91 offen offset:2048 lds
buffer_load_dwordx4 v27, s[32:35], s91 offen offset:3072 lds
s_add_u32 m0, s94, 135168
s_add_u32 s91, s91, 131072
buffer_load_dwordx4 v24, s[32:35], s91 offen offset:0 lds
buffer_load_dwordx4 v25, s[32:35], s91 offen offset:1024 lds
buffer_load_dwordx4 v26, s[32:35], s91 offen offset:2048 lds
buffer_load_dwordx4 v27, s[32:35], s91 offen offset:3072 lds
s_add_u32 m0, s94, 98304
s_bfe_u32 s91, s55, 0x40008
s_lshl_b32 s91, s91, 7
s_add_u32 s91, s91, s12
buffer_load_dwordx4 v24, s[32:35], s91 offen offset:0 lds
buffer_load_dwordx4 v25, s[32:35], s91 offen offset:1024 lds
buffer_load_dwordx4 v26, s[32:35], s91 offen offset:2048 lds
buffer_load_dwordx4 v27, s[32:35], s91 offen offset:3072 lds
s_add_u32 m0, s94, 102400
s_add_u32 s91, s91, 131072
buffer_load_dwordx4 v24, s[32:35], s91 offen offset:0 lds
buffer_load_dwordx4 v25, s[32:35], s91 offen offset:1024 lds
buffer_load_dwordx4 v26, s[32:35], s91 offen offset:2048 lds
buffer_load_dwordx4 v27, s[32:35], s91 offen offset:3072 lds
ds_read_b128 a[208:211], v28 offset:8192
ds_read_b128 a[240:243], v30 offset:8192
ds_read_b128 a[212:215], v28 offset:10240
ds_read_b128 a[244:247], v30 offset:10240
ds_read_b128 a[216:219], v29 offset:8192
ds_read_b128 a[248:251], v31 offset:8192
ds_read_b128 a[220:223], v29 offset:10240
ds_read_b128 a[252:255], v31 offset:10240
s_lshl_b32 s88, s24, 2
s_add_u32 s88, s88, s16
s_mul_i32 s30, s88, 61440
s_mul_i32 s88, s88, 61440
s_add_u32 s30, s30, 16908288
s_add_u32 s26, s88, 18874368
v_lshlrev_b32_e32 v19, 3, v0
s_lshl_b32 s27, s16, 9
s_add_u32 s27, s27, 8192
s_mul_i32 s83, s76, 81920
s_add_u32 s83, s83, 5373952
s_add_u32 s84, s83, 2621440
s_lshl_b32 s86, s17, 3
s_add_u32 s86, s86, 8192
s_or_b32 s87, s17, s16
s_add_u32 s48, s25, 0
s_and_b32 s48, s48, 3
s_add_u32 s49, s25, 1
s_and_b32 s49, s49, 3
s_add_u32 s50, s25, 2
s_and_b32 s50, s50, 3
s_add_u32 s51, s25, 3
s_and_b32 s51, s51, 3
s_lshl_b32 s88, s76, 2
s_add_u32 s88, s88, s74
s_mul_i32 s31, s88, 20480
s_lshl_b32 s88, s74, 15
s_mul_i32 s89, s75, 14336
s_add_u32 s90, s88, s89
s_add_u32 s90, s90, 32768
v_add_u32_e32 v20, s90, v1
s_sub_u32 s90, s89, s88
s_add_u32 s90, s90, 65536
v_add_u32_e32 v21, s90, v1
s_sub_u32 s90, s88, s89
s_add_u32 s90, s90, 47104
v_add_u32_e32 v22, s90, v1
s_add_u32 s90, s88, s89
s_sub_u32 s90, 79872, s90
v_add_u32_e32 v23, s90, v1
s_lshl_b32 s88, s16, 10
v_add_u32_e32 v247, s88, v1
v_add_u32_e32 v248, 16384, v247
s_lshl_b32 s88, s16, 11
v_add_u32_e32 v249, s88, v1
s_mul_i32 s88, s24, 384
s_mul_i32 s89, s16, 96
s_add_u32 s88, s88, s89
v_lshl_add_u32 v251, v3, 3, s88
v_mov_b32_e32 v60, 0x3f803f80
v_mov_b32_e32 v61, 0x3f803f80
v_mov_b32_e32 v62, 0x3f803f80
v_mov_b32_e32 v63, 0x3f803f80
s_lshl_b32 s88, s72, 4
s_lshl_b32 s89, s18, 3
s_add_u32 s88, s88, s89
s_add_u32 s88, s88, s73
s_lshl_b32 s88, s88, 3
s_add_u32 s78, s88, 16384
s_lshr_b32 s89, s76, 1
s_lshl_b32 s89, s89, 7
s_add_u32 s79, s89, 16384
s_lshl_b32 s88, s76, 7
s_add_u32 s88, s88, 12288
s_lshl_b32 s80, s24, 3
s_add_u32 s80, s80, s88
s_lshl_b32 s81, s16, 4
s_add_u32 s81, s81, s88
v_and_b32_e32 v250, 1, v0
v_lshlrev_b32_e32 v250, 3, v250
s_cmp_eq_u32 s16, 0
s_cselect_b32 s88, 0, 0x40000000
v_add_u32_e32 v246, s88, v246
buffer_load_dwordx2 v[244:245], v246, s[20:23], s53 offen sc1
s_add_u32 m0, s94, 65536
s_bfe_u32 s91, s55, 0x4000c
s_lshl_b32 s91, s91, 7
s_add_u32 s91, s91, s12
buffer_load_dwordx4 v24, s[32:35], s91 offen offset:0 lds
buffer_load_dwordx4 v25, s[32:35], s91 offen offset:1024 lds
buffer_load_dwordx4 v26, s[32:35], s91 offen offset:2048 lds
buffer_load_dwordx4 v27, s[32:35], s91 offen offset:3072 lds
s_add_u32 m0, s94, 69632
s_add_u32 s91, s91, 131072
buffer_load_dwordx4 v24, s[32:35], s91 offen offset:0 lds
buffer_load_dwordx4 v25, s[32:35], s91 offen offset:1024 lds
buffer_load_dwordx4 v26, s[32:35], s91 offen offset:2048 lds
buffer_load_dwordx4 v27, s[32:35], s91 offen offset:3072 lds
s_mov_b32 s36, s66
s_and_b32 s37, s67, 0xffff
s_mov_b32 s38, 2621440
s_mov_b32 s39, 0x20000
s_waitcnt lgkmcnt(0)
s_add_u32 m0, s94, 32768
s_bfe_u32 s91, s55, 0x40010
s_lshl_b32 s91, s91, 7
s_add_u32 s91, s91, s12
buffer_load_dwordx4 v24, s[32:35], s91 offen offset:0 lds
buffer_load_dwordx4 v25, s[32:35], s91 offen offset:1024 lds
buffer_load_dwordx4 v26, s[32:35], s91 offen offset:2048 lds
buffer_load_dwordx4 v27, s[32:35], s91 offen offset:3072 lds
s_add_u32 m0, s94, 36864
s_add_u32 s91, s91, 131072
buffer_load_dwordx4 v24, s[32:35], s91 offen offset:0 lds
buffer_load_dwordx4 v25, s[32:35], s91 offen offset:1024 lds
buffer_load_dwordx4 v26, s[32:35], s91 offen offset:2048 lds
buffer_load_dwordx4 v27, s[32:35], s91 offen offset:3072 lds
s_lshl_b32 s97, s19, 1
s_mul_i32 s97, s97, 10240
s_add_u32 s97, s97, s96
s_cmp_lg_u32 s16, 0
s_cbranch_scc1 Lpd2_%=
s_mov_b32 s8, 0x200000
s_waitcnt vmcnt(16)
v_cmp_ne_u64_e32 vcc, s[70:71], v[244:245]
s_cmp_eq_u64 vcc, 0
s_cbranch_scc1 Lpd2_%=
Lpl2_%=:
buffer_load_dwordx2 v[244:245], v246, s[20:23], s53 offen sc1
s_waitcnt vmcnt(0)
v_cmp_ne_u64_e32 vcc, s[70:71], v[244:245]
s_cmp_eq_u64 vcc, 0
s_cbranch_scc1 Lpd2_%=
s_sub_u32 s8, s8, 1
s_cbranch_scc0 Lpl2_%=
Lpd2_%=:
s_barrier
buffer_load_dwordx4 v[160:163], v16, s[20:23], s97 offen offset:0 sc1
buffer_load_dwordx4 v[164:167], v16, s[20:23], s97 offen offset:1024 sc1
buffer_load_dwordx4 v[168:171], v17, s[20:23], s97 offen offset:0 sc1
buffer_load_dwordx4 v[172:175], v18, s[20:23], s97 offen offset:0 sc1
buffer_load_dwordx4 v[176:179], v18, s[20:23], s97 offen offset:1024 sc1
s_add_u32 s97, s19, 1
s_and_b32 s97, s97, 7
s_lshl_b32 s97, s97, 1
s_mul_i32 s97, s97, 10240
s_add_u32 s97, s97, s96
buffer_load_dwordx4 v[180:183], v16, s[20:23], s97 offen offset:0 sc1
buffer_load_dwordx4 v[184:187], v16, s[20:23], s97 offen offset:1024 sc1
buffer_load_dwordx4 v[188:191], v17, s[20:23], s97 offen offset:0 sc1
buffer_load_dwordx4 v[192:195], v18, s[20:23], s97 offen offset:0 sc1
buffer_load_dwordx4 v[196:199], v18, s[20:23], s97 offen offset:1024 sc1
s_waitcnt vmcnt(7)
v_mfma_f32_16x16x32_bf16 v[144:147], v[168:171], a[208:211], 0
ds_read_b128 a[200:203], v29 offset:0
s_bfe_u32 s97, s55, 0x40008
s_mul_i32 s97, s97, 10240
s_add_u32 s97, s97, s96
v_mfma_f32_16x16x32_bf16 v[80:83], v[160:163], a[208:211], 0
buffer_load_dwordx4 v[200:203], v16, s[20:23], s97 offen offset:0 sc1
s_waitcnt vmcnt(7)
v_mfma_f32_16x16x32_bf16 v[80:83], v[172:175], a[240:243], v[80:83]
ds_read_b128 a[232:235], v31 offset:0
v_mfma_f32_16x16x32_bf16 v[112:115], v[164:167], a[208:211], 0
s_waitcnt vmcnt(6)
v_mfma_f32_16x16x32_bf16 v[112:115], v[176:179], a[240:243], v[112:115]
ds_read_b128 a[204:207], v29 offset:2048
v_mfma_f32_16x16x32_bf16 v[148:151], v[168:171], a[212:215], 0
buffer_load_dwordx4 v[204:207], v16, s[20:23], s97 offen offset:1024 sc1
)A"
R"A(v_mfma_f32_16x16x32_bf16 v[84:87], v[160:163], a[212:215], 0
ds_read_b128 a[236:239], v31 offset:2048
v_mfma_f32_16x16x32_bf16 v[84:87], v[172:175], a[244:247], v[84:87]
v_mfma_f32_16x16x32_bf16 v[116:119], v[164:167], a[212:215], 0
ds_read_b128 a[192:195], v28 offset:0
v_mfma_f32_16x16x32_bf16 v[116:119], v[176:179], a[244:247], v[116:119]
buffer_load_dwordx4 v[208:211], v17, s[20:23], s97 offen offset:0 sc1
v_mfma_f32_16x16x32_bf16 v[152:155], v[168:171], a[216:219], 0
ds_read_b128 a[224:227], v30 offset:0
v_mfma_f32_16x16x32_bf16 v[88:91], v[160:163], a[216:219], 0
v_mfma_f32_16x16x32_bf16 v[88:91], v[172:175], a[248:251], v[88:91]
ds_read_b128 a[196:199], v28 offset:2048
v_mfma_f32_16x16x32_bf16 v[120:123], v[164:167], a[216:219], 0
buffer_load_dwordx4 v[212:215], v18, s[20:23], s97 offen offset:0 sc1
v_mfma_f32_16x16x32_bf16 v[120:123], v[176:179], a[248:251], v[120:123]
ds_read_b128 a[228:231], v30 offset:2048
v_mfma_f32_16x16x32_bf16 v[156:159], v[168:171], a[220:223], 0
v_mfma_f32_16x16x32_bf16 v[92:95], v[160:163], a[220:223], 0
v_mfma_f32_16x16x32_bf16 v[92:95], v[172:175], a[252:255], v[92:95]
buffer_load_dwordx4 v[216:219], v18, s[20:23], s97 offen offset:1024 sc1
v_mfma_f32_16x16x32_bf16 v[124:127], v[164:167], a[220:223], 0
v_mfma_f32_16x16x32_bf16 v[124:127], v[176:179], a[252:255], v[124:127]
s_waitcnt lgkmcnt(0)
s_barrier
v_mfma_f32_16x16x32_bf16 v[136:139], v[168:171], a[200:203], 0
ds_read_b128 a[208:211], v36 offset:8192
s_add_u32 m0, s94, 0
s_bfe_u32 s91, s55, 0x40014
s_lshl_b32 s91, s91, 7
s_add_u32 s91, s91, s12
v_mfma_f32_16x16x32_bf16 v[72:75], v[160:163], a[200:203], v[72:75]
buffer_load_dwordx4 v24, s[32:35], s91 offen offset:0 lds
v_mfma_f32_16x16x32_bf16 v[72:75], v[172:175], a[232:235], v[72:75]
ds_read_b128 a[240:243], v38 offset:8192
v_mfma_f32_16x16x32_bf16 v[104:107], v[164:167], a[200:203], v[104:107]
buffer_load_dwordx4 v25, s[32:35], s91 offen offset:1024 lds
v_mfma_f32_16x16x32_bf16 v[104:107], v[176:179], a[232:235], v[104:107]
ds_read_b128 a[212:215], v36 offset:10240
v_mfma_f32_16x16x32_bf16 v[140:143], v[168:171], a[204:207], 0
buffer_load_dwordx4 v26, s[32:35], s91 offen offset:2048 lds
v_mfma_f32_16x16x32_bf16 v[76:79], v[160:163], a[204:207], v[76:79]
ds_read_b128 a[244:247], v38 offset:10240
v_mfma_f32_16x16x32_bf16 v[76:79], v[172:175], a[236:239], v[76:79]
v_mfma_f32_16x16x32_bf16 v[108:111], v[164:167], a[204:207], v[108:111]
ds_read_b128 a[216:219], v37 offset:8192
v_mfma_f32_16x16x32_bf16 v[108:111], v[176:179], a[236:239], v[108:111]
buffer_load_dwordx4 v27, s[32:35], s91 offen offset:3072 lds
v_mfma_f32_16x16x32_bf16 v[128:131], v[168:171], a[192:195], v[128:131]
ds_read_b128 a[248:251], v39 offset:8192
s_add_u32 m0, s94, 4096
s_add_u32 s91, s91, 131072
v_mfma_f32_16x16x32_bf16 v[64:67], v[160:163], a[192:195], v[64:67]
buffer_load_dwordx4 v24, s[32:35], s91 offen offset:0 lds
v_mfma_f32_16x16x32_bf16 v[64:67], v[172:175], a[224:227], v[64:67]
ds_read_b128 a[220:223], v37 offset:10240
v_mfma_f32_16x16x32_bf16 v[96:99], v[164:167], a[192:195], v[96:99]
v_mfma_f32_16x16x32_bf16 v[96:99], v[176:179], a[224:227], v[96:99]
ds_read_b128 a[252:255], v39 offset:10240
v_mfma_f32_16x16x32_bf16 v[132:135], v[168:171], a[196:199], v[132:135]
buffer_load_dwordx4 v25, s[32:35], s91 offen offset:1024 lds
v_mfma_f32_16x16x32_bf16 v[68:71], v[160:163], a[196:199], v[68:71]
v_mfma_f32_16x16x32_bf16 v[68:71], v[172:175], a[228:231], v[68:71]
buffer_load_dwordx4 v26, s[32:35], s91 offen offset:2048 lds
v_mfma_f32_16x16x32_bf16 v[100:103], v[164:167], a[196:199], v[100:103]
v_mfma_f32_16x16x32_bf16 v[100:103], v[176:179], a[228:231], v[100:103]
s_waitcnt vmcnt(14) lgkmcnt(7)
v_mfma_f32_16x16x32_bf16 v[144:147], v[188:191], a[208:211], v[144:147]
ds_read_b128 a[200:203], v37 offset:0
v_mfma_f32_16x16x32_bf16 v[80:83], v[180:183], a[208:211], v[80:83]
buffer_load_dwordx4 v27, s[32:35], s91 offen offset:3072 lds
s_waitcnt vmcnt(14) lgkmcnt(7)
v_mfma_f32_16x16x32_bf16 v[80:83], v[192:195], a[240:243], v[80:83]
ds_read_b128 a[232:235], v39 offset:0
s_bfe_u32 s97, s55, 0x4000c
s_mul_i32 s97, s97, 10240
s_add_u32 s97, s97, s96
v_mfma_f32_16x16x32_bf16 v[112:115], v[184:187], a[208:211], v[112:115]
buffer_load_dwordx4 v[220:223], v16, s[20:23], s97 offen offset:0 sc1
s_waitcnt vmcnt(14)
v_mfma_f32_16x16x32_bf16 v[112:115], v[196:199], a[240:243], v[112:115]
ds_read_b128 a[204:207], v37 offset:2048
s_waitcnt lgkmcnt(8)
v_mfma_f32_16x16x32_bf16 v[148:151], v[188:191], a[212:215], v[148:151]
v_mfma_f32_16x16x32_bf16 v[84:87], v[180:183], a[212:215], v[84:87]
ds_read_b128 a[236:239], v39 offset:2048
s_waitcnt lgkmcnt(8)
v_mfma_f32_16x16x32_bf16 v[84:87], v[192:195], a[244:247], v[84:87]
buffer_load_dwordx4 v[224:227], v16, s[20:23], s97 offen offset:1024 sc1
v_mfma_f32_16x16x32_bf16 v[116:119], v[184:187], a[212:215], v[116:119]
ds_read_b128 a[192:195], v36 offset:0
v_mfma_f32_16x16x32_bf16 v[116:119], v[196:199], a[244:247], v[116:119]
s_waitcnt lgkmcnt(8)
v_mfma_f32_16x16x32_bf16 v[152:155], v[188:191], a[216:219], v[152:155]
ds_read_b128 a[224:227], v38 offset:0
v_mfma_f32_16x16x32_bf16 v[88:91], v[180:183], a[216:219], v[88:91]
buffer_load_dwordx4 v[228:231], v17, s[20:23], s97 offen offset:0 sc1
s_waitcnt lgkmcnt(8)
v_mfma_f32_16x16x32_bf16 v[88:91], v[192:195], a[248:251], v[88:91]
ds_read_b128 a[196:199], v36 offset:2048
v_mfma_f32_16x16x32_bf16 v[120:123], v[184:187], a[216:219], v[120:123]
buffer_load_dwordx4 v[232:235], v18, s[20:23], s97 offen offset:0 sc1
v_mfma_f32_16x16x32_bf16 v[120:123], v[196:199], a[248:251], v[120:123]
ds_read_b128 a[228:231], v38 offset:2048
s_waitcnt lgkmcnt(9)
v_mfma_f32_16x16x32_bf16 v[156:159], v[188:191], a[220:223], v[156:159]
v_mfma_f32_16x16x32_bf16 v[92:95], v[180:183], a[220:223], v[92:95]
s_waitcnt lgkmcnt(8)
v_mfma_f32_16x16x32_bf16 v[92:95], v[192:195], a[252:255], v[92:95]
buffer_load_dwordx4 v[236:239], v18, s[20:23], s97 offen offset:1024 sc1
v_mfma_f32_16x16x32_bf16 v[124:127], v[184:187], a[220:223], v[124:127]
v_mfma_f32_16x16x32_bf16 v[124:127], v[196:199], a[252:255], v[124:127]
s_waitcnt lgkmcnt(0)
s_barrier
v_mfma_f32_16x16x32_bf16 v[136:139], v[188:191], a[200:203], v[136:139]
ds_read_b128 a[208:211], v32 offset:40960
s_add_u32 m0, s94, 131072
s_bfe_u32 s91, s55, 0x40018
s_lshl_b32 s91, s91, 7
s_add_u32 s91, s91, s12
v_mfma_f32_16x16x32_bf16 v[72:75], v[180:183], a[200:203], v[72:75]
buffer_load_dwordx4 v24, s[32:35], s91 offen offset:0 lds
v_mfma_f32_16x16x32_bf16 v[72:75], v[192:195], a[232:235], v[72:75]
ds_read_b128 a[240:243], v34 offset:40960
v_mfma_f32_16x16x32_bf16 v[104:107], v[184:187], a[200:203], v[104:107]
buffer_load_dwordx4 v25, s[32:35], s91 offen offset:1024 lds
v_mfma_f32_16x16x32_bf16 v[104:107], v[196:199], a[232:235], v[104:107]
ds_read_b128 a[212:215], v32 offset:43008
v_mfma_f32_16x16x32_bf16 v[140:143], v[188:191], a[204:207], v[140:143]
buffer_load_dwordx4 v26, s[32:35], s91 offen offset:2048 lds
v_mfma_f32_16x16x32_bf16 v[76:79], v[180:183], a[204:207], v[76:79]
ds_read_b128 a[244:247], v34 offset:43008
v_mfma_f32_16x16x32_bf16 v[76:79], v[192:195], a[236:239], v[76:79]
v_mfma_f32_16x16x32_bf16 v[108:111], v[184:187], a[204:207], v[108:111]
ds_read_b128 a[216:219], v33 offset:40960
v_mfma_f32_16x16x32_bf16 v[108:111], v[196:199], a[236:239], v[108:111]
buffer_load_dwordx4 v27, s[32:35], s91 offen offset:3072 lds
v_mfma_f32_16x16x32_bf16 v[128:131], v[188:191], a[192:195], v[128:131]
ds_read_b128 a[248:251], v35 offset:40960
s_add_u32 m0, s94, 135168
s_add_u32 s91, s91, 131072
v_mfma_f32_16x16x32_bf16 v[64:67], v[180:183], a[192:195], v[64:67]
buffer_load_dwordx4 v24, s[32:35], s91 offen offset:0 lds
v_mfma_f32_16x16x32_bf16 v[64:67], v[192:195], a[224:227], v[64:67]
ds_read_b128 a[220:223], v33 offset:43008
v_mfma_f32_16x16x32_bf16 v[96:99], v[184:187], a[192:195], v[96:99]
v_mfma_f32_16x16x32_bf16 v[96:99], v[196:199], a[224:227], v[96:99]
ds_read_b128 a[252:255], v35 offset:43008
v_mfma_f32_16x16x32_bf16 v[132:135], v[188:191], a[196:199], v[132:135]
)A"
R"A(buffer_load_dwordx4 v25, s[32:35], s91 offen offset:1024 lds
v_mfma_f32_16x16x32_bf16 v[68:71], v[180:183], a[196:199], v[68:71]
v_mfma_f32_16x16x32_bf16 v[68:71], v[192:195], a[228:231], v[68:71]
buffer_load_dwordx4 v26, s[32:35], s91 offen offset:2048 lds
v_mfma_f32_16x16x32_bf16 v[100:103], v[184:187], a[196:199], v[100:103]
v_mfma_f32_16x16x32_bf16 v[100:103], v[196:199], a[228:231], v[100:103]
s_waitcnt vmcnt(22) lgkmcnt(7)
v_mfma_f32_16x16x32_bf16 v[144:147], v[208:211], a[208:211], v[144:147]
ds_read_b128 a[200:203], v33 offset:32768
v_mfma_f32_16x16x32_bf16 v[80:83], v[200:203], a[208:211], v[80:83]
buffer_load_dwordx4 v27, s[32:35], s91 offen offset:3072 lds
s_waitcnt vmcnt(22) lgkmcnt(7)
v_mfma_f32_16x16x32_bf16 v[80:83], v[212:215], a[240:243], v[80:83]
ds_read_b128 a[232:235], v35 offset:32768
s_bfe_u32 s97, s55, 0x40010
s_mul_i32 s97, s97, 10240
s_add_u32 s97, s97, s96
v_mfma_f32_16x16x32_bf16 v[112:115], v[204:207], a[208:211], v[112:115]
buffer_load_dwordx4 v[40:43], v16, s[20:23], s97 offen offset:0 sc1
s_waitcnt vmcnt(22)
v_mfma_f32_16x16x32_bf16 v[112:115], v[216:219], a[240:243], v[112:115]
ds_read_b128 a[204:207], v33 offset:34816
s_waitcnt lgkmcnt(8)
v_mfma_f32_16x16x32_bf16 v[148:151], v[208:211], a[212:215], v[148:151]
v_mfma_f32_16x16x32_bf16 v[84:87], v[200:203], a[212:215], v[84:87]
ds_read_b128 a[236:239], v35 offset:34816
s_waitcnt lgkmcnt(8)
v_mfma_f32_16x16x32_bf16 v[84:87], v[212:215], a[244:247], v[84:87]
buffer_load_dwordx4 v[44:47], v16, s[20:23], s97 offen offset:1024 sc1
v_mfma_f32_16x16x32_bf16 v[116:119], v[204:207], a[212:215], v[116:119]
ds_read_b128 a[192:195], v32 offset:32768
v_mfma_f32_16x16x32_bf16 v[116:119], v[216:219], a[244:247], v[116:119]
s_waitcnt lgkmcnt(8)
v_mfma_f32_16x16x32_bf16 v[152:155], v[208:211], a[216:219], v[152:155]
ds_read_b128 a[224:227], v34 offset:32768
v_mfma_f32_16x16x32_bf16 v[88:91], v[200:203], a[216:219], v[88:91]
buffer_load_dwordx4 v[48:51], v17, s[20:23], s97 offen offset:0 sc1
s_waitcnt lgkmcnt(8)
v_mfma_f32_16x16x32_bf16 v[88:91], v[212:215], a[248:251], v[88:91]
ds_read_b128 a[196:199], v32 offset:34816
v_mfma_f32_16x16x32_bf16 v[120:123], v[204:207], a[216:219], v[120:123]
buffer_load_dwordx4 v[52:55], v18, s[20:23], s97 offen offset:0 sc1
v_mfma_f32_16x16x32_bf16 v[120:123], v[216:219], a[248:251], v[120:123]
ds_read_b128 a[228:231], v34 offset:34816
s_waitcnt lgkmcnt(9)
v_mfma_f32_16x16x32_bf16 v[156:159], v[208:211], a[220:223], v[156:159]
v_mfma_f32_16x16x32_bf16 v[92:95], v[200:203], a[220:223], v[92:95]
s_waitcnt lgkmcnt(8)
v_mfma_f32_16x16x32_bf16 v[92:95], v[212:215], a[252:255], v[92:95]
buffer_load_dwordx4 v[56:59], v18, s[20:23], s97 offen offset:1024 sc1
v_mfma_f32_16x16x32_bf16 v[124:127], v[204:207], a[220:223], v[124:127]
v_mfma_f32_16x16x32_bf16 v[124:127], v[216:219], a[252:255], v[124:127]
s_waitcnt lgkmcnt(0)
s_barrier
s_cmp_lg_u32 s16, 0
s_cbranch_scc1 Lpb3_%=
v_mov_b32_e32 v8, s70
v_mov_b32_e32 v9, s71
s_mov_b64 exec, 1
buffer_store_dwordx2 v[8:9], off, s[20:23], s86 sc1
s_mov_b64 exec, -1
Lpb3_%=:
v_mfma_f32_16x16x32_bf16 v[136:139], v[208:211], a[200:203], v[136:139]
ds_read_b128 a[208:211], v32 offset:8192
s_add_u32 m0, s94, 98304
s_bfe_u32 s91, s55, 0x4001c
s_lshl_b32 s91, s91, 7
s_add_u32 s91, s91, s12
v_mfma_f32_16x16x32_bf16 v[72:75], v[200:203], a[200:203], v[72:75]
buffer_load_dwordx4 v24, s[32:35], s91 offen offset:0 lds
v_mfma_f32_16x16x32_bf16 v[72:75], v[212:215], a[232:235], v[72:75]
ds_read_b128 a[240:243], v34 offset:8192
v_mfma_f32_16x16x32_bf16 v[104:107], v[204:207], a[200:203], v[104:107]
buffer_load_dwordx4 v25, s[32:35], s91 offen offset:1024 lds
v_mfma_f32_16x16x32_bf16 v[104:107], v[216:219], a[232:235], v[104:107]
ds_read_b128 a[212:215], v32 offset:10240
v_mfma_f32_16x16x32_bf16 v[140:143], v[208:211], a[204:207], v[140:143]
buffer_load_dwordx4 v26, s[32:35], s91 offen offset:2048 lds
v_mfma_f32_16x16x32_bf16 v[76:79], v[200:203], a[204:207], v[76:79]
ds_read_b128 a[244:247], v34 offset:10240
v_mfma_f32_16x16x32_bf16 v[76:79], v[212:215], a[236:239], v[76:79]
v_mfma_f32_16x16x32_bf16 v[108:111], v[204:207], a[204:207], v[108:111]
ds_read_b128 a[216:219], v33 offset:8192
v_mfma_f32_16x16x32_bf16 v[108:111], v[216:219], a[236:239], v[108:111]
buffer_load_dwordx4 v27, s[32:35], s91 offen offset:3072 lds
v_mfma_f32_16x16x32_bf16 v[128:131], v[208:211], a[192:195], v[128:131]
ds_read_b128 a[248:251], v35 offset:8192
s_add_u32 m0, s94, 102400
s_add_u32 s91, s91, 131072
v_mfma_f32_16x16x32_bf16 v[64:67], v[200:203], a[192:195], v[64:67]
buffer_load_dwordx4 v24, s[32:35], s91 offen offset:0 lds
v_mfma_f32_16x16x32_bf16 v[64:67], v[212:215], a[224:227], v[64:67]
ds_read_b128 a[220:223], v33 offset:10240
v_mfma_f32_16x16x32_bf16 v[96:99], v[204:207], a[192:195], v[96:99]
v_mfma_f32_16x16x32_bf16 v[96:99], v[216:219], a[224:227], v[96:99]
ds_read_b128 a[252:255], v35 offset:10240
v_mfma_f32_16x16x32_bf16 v[132:135], v[208:211], a[196:199], v[132:135]
buffer_load_dwordx4 v25, s[32:35], s91 offen offset:1024 lds
v_mfma_f32_16x16x32_bf16 v[68:71], v[200:203], a[196:199], v[68:71]
v_mfma_f32_16x16x32_bf16 v[68:71], v[212:215], a[228:231], v[68:71]
buffer_load_dwordx4 v26, s[32:35], s91 offen offset:2048 lds
v_mfma_f32_16x16x32_bf16 v[100:103], v[204:207], a[196:199], v[100:103]
v_mfma_f32_16x16x32_bf16 v[100:103], v[216:219], a[228:231], v[100:103]
s_waitcnt vmcnt(22) lgkmcnt(7)
v_mfma_f32_16x16x32_bf16 v[144:147], v[228:231], a[208:211], v[144:147]
ds_read_b128 a[200:203], v33 offset:0
v_mfma_f32_16x16x32_bf16 v[80:83], v[220:223], a[208:211], v[80:83]
buffer_load_dwordx4 v27, s[32:35], s91 offen offset:3072 lds
s_waitcnt vmcnt(22) lgkmcnt(7)
v_mfma_f32_16x16x32_bf16 v[80:83], v[232:235], a[240:243], v[80:83]
ds_read_b128 a[232:235], v35 offset:0
s_bfe_u32 s97, s55, 0x40014
s_mul_i32 s97, s97, 10240
s_add_u32 s97, s97, s96
v_mfma_f32_16x16x32_bf16 v[112:115], v[224:227], a[208:211], v[112:115]
buffer_load_dwordx4 v[160:163], v16, s[20:23], s97 offen offset:0 sc1
s_waitcnt vmcnt(22)
v_mfma_f32_16x16x32_bf16 v[112:115], v[236:239], a[240:243], v[112:115]
ds_read_b128 a[204:207], v33 offset:2048
s_waitcnt lgkmcnt(8)
v_mfma_f32_16x16x32_bf16 v[148:151], v[228:231], a[212:215], v[148:151]
v_mfma_f32_16x16x32_bf16 v[84:87], v[220:223], a[212:215], v[84:87]
ds_read_b128 a[236:239], v35 offset:2048
s_waitcnt lgkmcnt(8)
v_mfma_f32_16x16x32_bf16 v[84:87], v[232:235], a[244:247], v[84:87]
buffer_load_dwordx4 v[164:167], v16, s[20:23], s97 offen offset:1024 sc1
v_mfma_f32_16x16x32_bf16 v[116:119], v[224:227], a[212:215], v[116:119]
ds_read_b128 a[192:195], v32 offset:0
v_mfma_f32_16x16x32_bf16 v[116:119], v[236:239], a[244:247], v[116:119]
s_waitcnt lgkmcnt(8)
v_mfma_f32_16x16x32_bf16 v[152:155], v[228:231], a[216:219], v[152:155]
ds_read_b128 a[224:227], v34 offset:0
v_mfma_f32_16x16x32_bf16 v[88:91], v[220:223], a[216:219], v[88:91]
buffer_load_dwordx4 v[168:171], v17, s[20:23], s97 offen offset:0 sc1
s_waitcnt lgkmcnt(8)
v_mfma_f32_16x16x32_bf16 v[88:91], v[232:235], a[248:251], v[88:91]
ds_read_b128 a[196:199], v32 offset:2048
v_mfma_f32_16x16x32_bf16 v[120:123], v[224:227], a[216:219], v[120:123]
buffer_load_dwordx4 v[172:175], v18, s[20:23], s97 offen offset:0 sc1
v_mfma_f32_16x16x32_bf16 v[120:123], v[236:239], a[248:251], v[120:123]
ds_read_b128 a[228:231], v34 offset:2048
s_waitcnt lgkmcnt(9)
v_mfma_f32_16x16x32_bf16 v[156:159], v[228:231], a[220:223], v[156:159]
v_mfma_f32_16x16x32_bf16 v[92:95], v[220:223], a[220:223], v[92:95]
s_waitcnt lgkmcnt(8)
v_mfma_f32_16x16x32_bf16 v[92:95], v[232:235], a[252:255], v[92:95]
buffer_load_dwordx4 v[176:179], v18, s[20:23], s97 offen offset:1024 sc1
v_mfma_f32_16x16x32_bf16 v[124:127], v[224:227], a[220:223], v[124:127]
v_mfma_f32_16x16x32_bf16 v[124:127], v[236:239], a[252:255], v[124:127]
s_waitcnt lgkmcnt(0)
s_barrier
v_mfma_f32_16x16x32_bf16 v[136:139], v[228:231], a[200:203], v[136:139]
ds_read_b128 a[208:211], v28 offset:40960
s_add_u32 m0, s94, 65536
s_bfe_u32 s91, s9, 0x40000
s_lshl_b32 s91, s91, 7
)A"
R"A(s_add_u32 s91, s91, s12
v_mfma_f32_16x16x32_bf16 v[72:75], v[220:223], a[200:203], v[72:75]
buffer_load_dwordx4 v24, s[32:35], s91 offen offset:0 lds
v_mfma_f32_16x16x32_bf16 v[72:75], v[232:235], a[232:235], v[72:75]
ds_read_b128 a[240:243], v30 offset:40960
v_mfma_f32_16x16x32_bf16 v[104:107], v[224:227], a[200:203], v[104:107]
buffer_load_dwordx4 v25, s[32:35], s91 offen offset:1024 lds
v_mfma_f32_16x16x32_bf16 v[104:107], v[236:239], a[232:235], v[104:107]
ds_read_b128 a[212:215], v28 offset:43008
v_mfma_f32_16x16x32_bf16 v[140:143], v[228:231], a[204:207], v[140:143]
buffer_load_dwordx4 v26, s[32:35], s91 offen offset:2048 lds
v_mfma_f32_16x16x32_bf16 v[76:79], v[220:223], a[204:207], v[76:79]
ds_read_b128 a[244:247], v30 offset:43008
v_mfma_f32_16x16x32_bf16 v[76:79], v[232:235], a[236:239], v[76:79]
v_mfma_f32_16x16x32_bf16 v[108:111], v[224:227], a[204:207], v[108:111]
ds_read_b128 a[216:219], v29 offset:40960
v_mfma_f32_16x16x32_bf16 v[108:111], v[236:239], a[236:239], v[108:111]
buffer_load_dwordx4 v27, s[32:35], s91 offen offset:3072 lds
v_mfma_f32_16x16x32_bf16 v[128:131], v[228:231], a[192:195], v[128:131]
ds_read_b128 a[248:251], v31 offset:40960
s_add_u32 m0, s94, 69632
s_add_u32 s91, s91, 131072
v_mfma_f32_16x16x32_bf16 v[64:67], v[220:223], a[192:195], v[64:67]
buffer_load_dwordx4 v24, s[32:35], s91 offen offset:0 lds
v_mfma_f32_16x16x32_bf16 v[64:67], v[232:235], a[224:227], v[64:67]
ds_read_b128 a[220:223], v29 offset:43008
v_mfma_f32_16x16x32_bf16 v[96:99], v[224:227], a[192:195], v[96:99]
v_mfma_f32_16x16x32_bf16 v[96:99], v[236:239], a[224:227], v[96:99]
ds_read_b128 a[252:255], v31 offset:43008
v_mfma_f32_16x16x32_bf16 v[132:135], v[228:231], a[196:199], v[132:135]
buffer_load_dwordx4 v25, s[32:35], s91 offen offset:1024 lds
v_mfma_f32_16x16x32_bf16 v[68:71], v[220:223], a[196:199], v[68:71]
v_mfma_f32_16x16x32_bf16 v[68:71], v[232:235], a[228:231], v[68:71]
buffer_load_dwordx4 v26, s[32:35], s91 offen offset:2048 lds
v_mfma_f32_16x16x32_bf16 v[100:103], v[224:227], a[196:199], v[100:103]
v_mfma_f32_16x16x32_bf16 v[100:103], v[236:239], a[228:231], v[100:103]
s_waitcnt vmcnt(22) lgkmcnt(7)
v_mfma_f32_16x16x32_bf16 v[144:147], v[48:51], a[208:211], v[144:147]
ds_read_b128 a[200:203], v29 offset:32768
v_mfma_f32_16x16x32_bf16 v[80:83], v[40:43], a[208:211], v[80:83]
buffer_load_dwordx4 v27, s[32:35], s91 offen offset:3072 lds
s_waitcnt vmcnt(22) lgkmcnt(7)
v_mfma_f32_16x16x32_bf16 v[80:83], v[52:55], a[240:243], v[80:83]
ds_read_b128 a[232:235], v31 offset:32768
s_bfe_u32 s97, s55, 0x40018
s_mul_i32 s97, s97, 10240
s_add_u32 s97, s97, s96
v_mfma_f32_16x16x32_bf16 v[112:115], v[44:47], a[208:211], v[112:115]
buffer_load_dwordx4 v[180:183], v16, s[20:23], s97 offen offset:0 sc1
s_waitcnt vmcnt(22)
v_mfma_f32_16x16x32_bf16 v[112:115], v[56:59], a[240:243], v[112:115]
ds_read_b128 a[204:207], v29 offset:34816
s_waitcnt lgkmcnt(8)
v_mfma_f32_16x16x32_bf16 v[148:151], v[48:51], a[212:215], v[148:151]
v_mfma_f32_16x16x32_bf16 v[84:87], v[40:43], a[212:215], v[84:87]
ds_read_b128 a[236:239], v31 offset:34816
s_waitcnt lgkmcnt(8)
v_mfma_f32_16x16x32_bf16 v[84:87], v[52:55], a[244:247], v[84:87]
buffer_load_dwordx4 v[184:187], v16, s[20:23], s97 offen offset:1024 sc1
v_mfma_f32_16x16x32_bf16 v[116:119], v[44:47], a[212:215], v[116:119]
ds_read_b128 a[192:195], v28 offset:32768
v_mfma_f32_16x16x32_bf16 v[116:119], v[56:59], a[244:247], v[116:119]
s_waitcnt lgkmcnt(8)
v_mfma_f32_16x16x32_bf16 v[152:155], v[48:51], a[216:219], v[152:155]
ds_read_b128 a[224:227], v30 offset:32768
v_mfma_f32_16x16x32_bf16 v[88:91], v[40:43], a[216:219], v[88:91]
buffer_load_dwordx4 v[188:191], v17, s[20:23], s97 offen offset:0 sc1
s_waitcnt lgkmcnt(8)
v_mfma_f32_16x16x32_bf16 v[88:91], v[52:55], a[248:251], v[88:91]
ds_read_b128 a[196:199], v28 offset:34816
v_mfma_f32_16x16x32_bf16 v[120:123], v[44:47], a[216:219], v[120:123]
buffer_load_dwordx4 v[192:195], v18, s[20:23], s97 offen offset:0 sc1
v_mfma_f32_16x16x32_bf16 v[120:123], v[56:59], a[248:251], v[120:123]
ds_read_b128 a[228:231], v30 offset:34816
s_waitcnt lgkmcnt(9)
v_mfma_f32_16x16x32_bf16 v[156:159], v[48:51], a[220:223], v[156:159]
v_mfma_f32_16x16x32_bf16 v[92:95], v[40:43], a[220:223], v[92:95]
s_waitcnt lgkmcnt(8)
v_mfma_f32_16x16x32_bf16 v[92:95], v[52:55], a[252:255], v[92:95]
buffer_load_dwordx4 v[196:199], v18, s[20:23], s97 offen offset:1024 sc1
v_mfma_f32_16x16x32_bf16 v[124:127], v[44:47], a[220:223], v[124:127]
v_mfma_f32_16x16x32_bf16 v[124:127], v[56:59], a[252:255], v[124:127]
s_waitcnt lgkmcnt(0)
s_barrier
v_mfma_f32_16x16x32_bf16 v[136:139], v[48:51], a[200:203], v[136:139]
ds_read_b128 a[208:211], v28 offset:8192
s_add_u32 m0, s94, 32768
s_bfe_u32 s91, s9, 0x40004
s_lshl_b32 s91, s91, 7
s_add_u32 s91, s91, s12
v_mfma_f32_16x16x32_bf16 v[72:75], v[40:43], a[200:203], v[72:75]
buffer_load_dwordx4 v24, s[32:35], s91 offen offset:0 lds
v_mfma_f32_16x16x32_bf16 v[72:75], v[52:55], a[232:235], v[72:75]
ds_read_b128 a[240:243], v30 offset:8192
v_mfma_f32_16x16x32_bf16 v[104:107], v[44:47], a[200:203], v[104:107]
buffer_load_dwordx4 v25, s[32:35], s91 offen offset:1024 lds
v_mfma_f32_16x16x32_bf16 v[104:107], v[56:59], a[232:235], v[104:107]
ds_read_b128 a[212:215], v28 offset:10240
v_mfma_f32_16x16x32_bf16 v[140:143], v[48:51], a[204:207], v[140:143]
buffer_load_dwordx4 v26, s[32:35], s91 offen offset:2048 lds
v_mfma_f32_16x16x32_bf16 v[76:79], v[40:43], a[204:207], v[76:79]
ds_read_b128 a[244:247], v30 offset:10240
v_mfma_f32_16x16x32_bf16 v[76:79], v[52:55], a[236:239], v[76:79]
v_mfma_f32_16x16x32_bf16 v[108:111], v[44:47], a[204:207], v[108:111]
ds_read_b128 a[216:219], v29 offset:8192
v_mfma_f32_16x16x32_bf16 v[108:111], v[56:59], a[236:239], v[108:111]
buffer_load_dwordx4 v27, s[32:35], s91 offen offset:3072 lds
v_mfma_f32_16x16x32_bf16 v[128:131], v[48:51], a[192:195], v[128:131]
ds_read_b128 a[248:251], v31 offset:8192
s_add_u32 m0, s94, 36864
s_add_u32 s91, s91, 131072
v_mfma_f32_16x16x32_bf16 v[64:67], v[40:43], a[192:195], v[64:67]
buffer_load_dwordx4 v24, s[32:35], s91 offen offset:0 lds
v_mfma_f32_16x16x32_bf16 v[64:67], v[52:55], a[224:227], v[64:67]
ds_read_b128 a[220:223], v29 offset:10240
v_mfma_f32_16x16x32_bf16 v[96:99], v[44:47], a[192:195], v[96:99]
v_mfma_f32_16x16x32_bf16 v[96:99], v[56:59], a[224:227], v[96:99]
ds_read_b128 a[252:255], v31 offset:10240
v_mfma_f32_16x16x32_bf16 v[132:135], v[48:51], a[196:199], v[132:135]
buffer_load_dwordx4 v25, s[32:35], s91 offen offset:1024 lds
v_mfma_f32_16x16x32_bf16 v[68:71], v[40:43], a[196:199], v[68:71]
v_mfma_f32_16x16x32_bf16 v[68:71], v[52:55], a[228:231], v[68:71]
buffer_load_dwordx4 v26, s[32:35], s91 offen offset:2048 lds
v_mfma_f32_16x16x32_bf16 v[100:103], v[44:47], a[196:199], v[100:103]
v_mfma_f32_16x16x32_bf16 v[100:103], v[56:59], a[228:231], v[100:103]
s_waitcnt vmcnt(22) lgkmcnt(7)
v_mfma_f32_16x16x32_bf16 v[144:147], v[168:171], a[208:211], v[144:147]
ds_read_b128 a[200:203], v29 offset:0
v_mfma_f32_16x16x32_bf16 v[80:83], v[160:163], a[208:211], v[80:83]
buffer_load_dwordx4 v27, s[32:35], s91 offen offset:3072 lds
s_waitcnt vmcnt(22) lgkmcnt(7)
v_mfma_f32_16x16x32_bf16 v[80:83], v[172:175], a[240:243], v[80:83]
ds_read_b128 a[232:235], v31 offset:0
s_bfe_u32 s97, s55, 0x4001c
s_mul_i32 s97, s97, 10240
s_add_u32 s97, s97, s96
v_mfma_f32_16x16x32_bf16 v[112:115], v[164:167], a[208:211], v[112:115]
buffer_load_dwordx4 v[200:203], v16, s[20:23], s97 offen offset:0 sc1
s_waitcnt vmcnt(22)
v_mfma_f32_16x16x32_bf16 v[112:115], v[176:179], a[240:243], v[112:115]
ds_read_b128 a[204:207], v29 offset:2048
s_waitcnt lgkmcnt(8)
v_mfma_f32_16x16x32_bf16 v[148:151], v[168:171], a[212:215], v[148:151]
buffer_load_dwordx4 v[204:207], v16, s[20:23], s97 offen offset:1024 sc1
v_mfma_f32_16x16x32_bf16 v[84:87], v[160:163], a[212:215], v[84:87]
ds_read_b128 a[236:239], v31 offset:2048
s_waitcnt lgkmcnt(8)
v_mfma_f32_16x16x32_bf16 v[84:87], v[172:175], a[244:247], v[84:87]
v_mfma_f32_16x16x32_bf16 v[116:119], v[164:167], a[212:215], v[116:119]
)A"
R"A(ds_read_b128 a[192:195], v28 offset:0
v_mfma_f32_16x16x32_bf16 v[116:119], v[176:179], a[244:247], v[116:119]
buffer_load_dwordx4 v[208:211], v17, s[20:23], s97 offen offset:0 sc1
s_waitcnt lgkmcnt(8)
v_mfma_f32_16x16x32_bf16 v[152:155], v[168:171], a[216:219], v[152:155]
ds_read_b128 a[224:227], v30 offset:0
v_mfma_f32_16x16x32_bf16 v[88:91], v[160:163], a[216:219], v[88:91]
buffer_load_dwordx4 v[212:215], v18, s[20:23], s97 offen offset:0 sc1
s_waitcnt lgkmcnt(8)
v_mfma_f32_16x16x32_bf16 v[88:91], v[172:175], a[248:251], v[88:91]
ds_read_b128 a[196:199], v28 offset:2048
v_mfma_f32_16x16x32_bf16 v[120:123], v[164:167], a[216:219], v[120:123]
v_mfma_f32_16x16x32_bf16 v[120:123], v[176:179], a[248:251], v[120:123]
ds_read_b128 a[228:231], v30 offset:2048
s_waitcnt lgkmcnt(9)
v_mfma_f32_16x16x32_bf16 v[156:159], v[168:171], a[220:223], v[156:159]
buffer_load_dwordx4 v[216:219], v18, s[20:23], s97 offen offset:1024 sc1
v_mfma_f32_16x16x32_bf16 v[92:95], v[160:163], a[220:223], v[92:95]
s_waitcnt lgkmcnt(8)
v_mfma_f32_16x16x32_bf16 v[92:95], v[172:175], a[252:255], v[92:95]
buffer_load_dwordx2 v[244:245], v19, s[20:23], s27 offen sc1
v_mfma_f32_16x16x32_bf16 v[124:127], v[164:167], a[220:223], v[124:127]
v_mfma_f32_16x16x32_bf16 v[124:127], v[176:179], a[252:255], v[124:127]
s_waitcnt lgkmcnt(0)
s_barrier
v_mfma_f32_16x16x32_bf16 v[136:139], v[168:171], a[200:203], v[136:139]
ds_read_b128 a[208:211], v36 offset:8192
s_add_u32 m0, s94, 0
s_bfe_u32 s91, s9, 0x40008
s_lshl_b32 s91, s91, 7
s_add_u32 s91, s91, s12
v_mfma_f32_16x16x32_bf16 v[72:75], v[160:163], a[200:203], v[72:75]
buffer_load_dwordx4 v24, s[32:35], s91 offen offset:0 lds
v_mfma_f32_16x16x32_bf16 v[72:75], v[172:175], a[232:235], v[72:75]
ds_read_b128 a[240:243], v38 offset:8192
v_mfma_f32_16x16x32_bf16 v[104:107], v[164:167], a[200:203], v[104:107]
buffer_load_dwordx4 v25, s[32:35], s91 offen offset:1024 lds
v_mfma_f32_16x16x32_bf16 v[104:107], v[176:179], a[232:235], v[104:107]
ds_read_b128 a[212:215], v36 offset:10240
v_mfma_f32_16x16x32_bf16 v[140:143], v[168:171], a[204:207], v[140:143]
buffer_load_dwordx4 v26, s[32:35], s91 offen offset:2048 lds
v_mfma_f32_16x16x32_bf16 v[76:79], v[160:163], a[204:207], v[76:79]
ds_read_b128 a[244:247], v38 offset:10240
v_mfma_f32_16x16x32_bf16 v[76:79], v[172:175], a[236:239], v[76:79]
v_mfma_f32_16x16x32_bf16 v[108:111], v[164:167], a[204:207], v[108:111]
ds_read_b128 a[216:219], v37 offset:8192
v_mfma_f32_16x16x32_bf16 v[108:111], v[176:179], a[236:239], v[108:111]
buffer_load_dwordx4 v27, s[32:35], s91 offen offset:3072 lds
v_mfma_f32_16x16x32_bf16 v[128:131], v[168:171], a[192:195], v[128:131]
ds_read_b128 a[248:251], v39 offset:8192
s_add_u32 m0, s94, 4096
s_add_u32 s91, s91, 131072
v_mfma_f32_16x16x32_bf16 v[64:67], v[160:163], a[192:195], v[64:67]
buffer_load_dwordx4 v24, s[32:35], s91 offen offset:0 lds
v_mfma_f32_16x16x32_bf16 v[64:67], v[172:175], a[224:227], v[64:67]
ds_read_b128 a[220:223], v37 offset:10240
v_mfma_f32_16x16x32_bf16 v[96:99], v[164:167], a[192:195], v[96:99]
v_mfma_f32_16x16x32_bf16 v[96:99], v[176:179], a[224:227], v[96:99]
ds_read_b128 a[252:255], v39 offset:10240
v_mfma_f32_16x16x32_bf16 v[132:135], v[168:171], a[196:199], v[132:135]
buffer_load_dwordx4 v25, s[32:35], s91 offen offset:1024 lds
v_mfma_f32_16x16x32_bf16 v[68:71], v[160:163], a[196:199], v[68:71]
v_mfma_f32_16x16x32_bf16 v[68:71], v[172:175], a[228:231], v[68:71]
buffer_load_dwordx4 v26, s[32:35], s91 offen offset:2048 lds
v_mfma_f32_16x16x32_bf16 v[100:103], v[164:167], a[196:199], v[100:103]
v_mfma_f32_16x16x32_bf16 v[100:103], v[176:179], a[228:231], v[100:103]
s_waitcnt vmcnt(23) lgkmcnt(7)
v_mfma_f32_16x16x32_bf16 v[144:147], v[188:191], a[208:211], v[144:147]
ds_read_b128 a[200:203], v37 offset:0
v_mfma_f32_16x16x32_bf16 v[80:83], v[180:183], a[208:211], v[80:83]
buffer_load_dwordx4 v27, s[32:35], s91 offen offset:3072 lds
s_waitcnt vmcnt(23) lgkmcnt(7)
v_mfma_f32_16x16x32_bf16 v[80:83], v[192:195], a[240:243], v[80:83]
ds_read_b128 a[232:235], v39 offset:0
s_bfe_u32 s97, s9, 0x40000
s_mul_i32 s97, s97, 10240
s_add_u32 s97, s97, s96
v_mfma_f32_16x16x32_bf16 v[112:115], v[184:187], a[208:211], v[112:115]
buffer_load_dwordx4 v[220:223], v16, s[20:23], s97 offen offset:0 sc1
s_waitcnt vmcnt(23)
v_mfma_f32_16x16x32_bf16 v[112:115], v[196:199], a[240:243], v[112:115]
ds_read_b128 a[204:207], v37 offset:2048
s_waitcnt lgkmcnt(8)
v_mfma_f32_16x16x32_bf16 v[148:151], v[188:191], a[212:215], v[148:151]
v_mfma_f32_16x16x32_bf16 v[84:87], v[180:183], a[212:215], v[84:87]
ds_read_b128 a[236:239], v39 offset:2048
s_waitcnt lgkmcnt(8)
v_mfma_f32_16x16x32_bf16 v[84:87], v[192:195], a[244:247], v[84:87]
buffer_load_dwordx4 v[224:227], v16, s[20:23], s97 offen offset:1024 sc1
v_mfma_f32_16x16x32_bf16 v[116:119], v[184:187], a[212:215], v[116:119]
ds_read_b128 a[192:195], v36 offset:0
v_mfma_f32_16x16x32_bf16 v[116:119], v[196:199], a[244:247], v[116:119]
s_waitcnt lgkmcnt(8)
v_mfma_f32_16x16x32_bf16 v[152:155], v[188:191], a[216:219], v[152:155]
ds_read_b128 a[224:227], v38 offset:0
v_mfma_f32_16x16x32_bf16 v[88:91], v[180:183], a[216:219], v[88:91]
buffer_load_dwordx4 v[228:231], v17, s[20:23], s97 offen offset:0 sc1
s_waitcnt lgkmcnt(8)
v_mfma_f32_16x16x32_bf16 v[88:91], v[192:195], a[248:251], v[88:91]
ds_read_b128 a[196:199], v36 offset:2048
v_mfma_f32_16x16x32_bf16 v[120:123], v[184:187], a[216:219], v[120:123]
buffer_load_dwordx4 v[232:235], v18, s[20:23], s97 offen offset:0 sc1
v_mfma_f32_16x16x32_bf16 v[120:123], v[196:199], a[248:251], v[120:123]
ds_read_b128 a[228:231], v38 offset:2048
s_waitcnt lgkmcnt(9)
v_mfma_f32_16x16x32_bf16 v[156:159], v[188:191], a[220:223], v[156:159]
v_mfma_f32_16x16x32_bf16 v[92:95], v[180:183], a[220:223], v[92:95]
s_waitcnt lgkmcnt(8)
v_mfma_f32_16x16x32_bf16 v[92:95], v[192:195], a[252:255], v[92:95]
buffer_load_dwordx4 v[236:239], v18, s[20:23], s97 offen offset:1024 sc1
v_mfma_f32_16x16x32_bf16 v[124:127], v[184:187], a[220:223], v[124:127]
v_mfma_f32_16x16x32_bf16 v[124:127], v[196:199], a[252:255], v[124:127]
s_waitcnt vmcnt(13)
v_cmp_ne_u64_e32 vcc, s[70:71], v[244:245]
s_mov_b32 s8, 0x200000
s_cmp_eq_u64 vcc, 0
s_cbranch_scc1 Lfb4_%=
Lfs4_%=:
buffer_load_dwordx2 v[244:245], v19, s[20:23], s27 offen sc1
s_waitcnt vmcnt(0)
v_cmp_ne_u64_e32 vcc, s[70:71], v[244:245]
s_cmp_eq_u64 vcc, 0
s_cbranch_scc1 Lfb4_%=
s_sub_u32 s8, s8, 1
s_cbranch_scc0 Lfs4_%=
Lfb4_%=:
s_waitcnt lgkmcnt(0)
s_barrier
s_cmp_lg_u32 s87, 0
s_cbranch_scc1 Lpb5_%=
v_mov_b32_e32 v8, s70
v_mov_b32_e32 v9, s71
s_mov_b64 exec, 1
buffer_store_dwordx2 v[8:9], off, s[20:23], 0 sc1
s_mov_b64 exec, -1
Lpb5_%=:
v_mfma_f32_16x16x32_bf16 v[136:139], v[188:191], a[200:203], v[136:139]
ds_read_b128 a[208:211], v32 offset:40960
s_add_u32 m0, s94, 131072
s_bfe_u32 s91, s9, 0x4000c
s_lshl_b32 s91, s91, 7
s_add_u32 s91, s91, s12
v_mfma_f32_16x16x32_bf16 v[72:75], v[180:183], a[200:203], v[72:75]
buffer_load_dwordx4 v24, s[32:35], s91 offen offset:0 lds
v_mfma_f32_16x16x32_bf16 v[72:75], v[192:195], a[232:235], v[72:75]
ds_read_b128 a[240:243], v34 offset:40960
v_mfma_f32_16x16x32_bf16 v[104:107], v[184:187], a[200:203], v[104:107]
buffer_load_dwordx4 v25, s[32:35], s91 offen offset:1024 lds
v_mfma_f32_16x16x32_bf16 v[104:107], v[196:199], a[232:235], v[104:107]
ds_read_b128 a[212:215], v32 offset:43008
v_mfma_f32_16x16x32_bf16 v[140:143], v[188:191], a[204:207], v[140:143]
buffer_load_dwordx4 v26, s[32:35], s91 offen offset:2048 lds
v_mfma_f32_16x16x32_bf16 v[76:79], v[180:183], a[204:207], v[76:79]
ds_read_b128 a[244:247], v34 offset:43008
v_mfma_f32_16x16x32_bf16 v[76:79], v[192:195], a[236:239], v[76:79]
v_mfma_f32_16x16x32_bf16 v[108:111], v[184:187], a[204:207], v[108:111]
ds_read_b128 a[216:219], v33 offset:40960
v_mfma_f32_16x16x32_bf16 v[108:111], v[196:199], a[236:239], v[108:111]
buffer_load_dwordx4 v27, s[32:35], s91 offen offset:3072 lds
v_mfma_f32_16x16x32_bf16 v[128:131], v[188:191], a[192:195], v[128:131]
ds_read_b128 a[248:251], v35 offset:40960
s_add_u32 m0, s94, 135168
)A"
R"A(s_add_u32 s91, s91, 131072
v_mfma_f32_16x16x32_bf16 v[64:67], v[180:183], a[192:195], v[64:67]
buffer_load_dwordx4 v24, s[32:35], s91 offen offset:0 lds
v_mfma_f32_16x16x32_bf16 v[64:67], v[192:195], a[224:227], v[64:67]
ds_read_b128 a[220:223], v33 offset:43008
v_mfma_f32_16x16x32_bf16 v[96:99], v[184:187], a[192:195], v[96:99]
v_mfma_f32_16x16x32_bf16 v[96:99], v[196:199], a[224:227], v[96:99]
ds_read_b128 a[252:255], v35 offset:43008
v_mfma_f32_16x16x32_bf16 v[132:135], v[188:191], a[196:199], v[132:135]
buffer_load_dwordx4 v25, s[32:35], s91 offen offset:1024 lds
v_mfma_f32_16x16x32_bf16 v[68:71], v[180:183], a[196:199], v[68:71]
v_mfma_f32_16x16x32_bf16 v[68:71], v[192:195], a[228:231], v[68:71]
buffer_load_dwordx4 v26, s[32:35], s91 offen offset:2048 lds
v_mfma_f32_16x16x32_bf16 v[100:103], v[184:187], a[196:199], v[100:103]
v_mfma_f32_16x16x32_bf16 v[100:103], v[196:199], a[228:231], v[100:103]
s_waitcnt lgkmcnt(7)
v_mfma_f32_16x16x32_bf16 v[144:147], v[208:211], a[208:211], v[144:147]
ds_read_b128 a[200:203], v33 offset:32768
v_mfma_f32_16x16x32_bf16 v[80:83], v[200:203], a[208:211], v[80:83]
buffer_load_dwordx4 v27, s[32:35], s91 offen offset:3072 lds
s_waitcnt lgkmcnt(7)
v_mfma_f32_16x16x32_bf16 v[80:83], v[212:215], a[240:243], v[80:83]
ds_read_b128 a[232:235], v35 offset:32768
s_bfe_u32 s97, s9, 0x40004
s_mul_i32 s97, s97, 10240
s_add_u32 s97, s97, s96
v_mfma_f32_16x16x32_bf16 v[112:115], v[204:207], a[208:211], v[112:115]
buffer_load_dwordx4 v[40:43], v16, s[20:23], s97 offen offset:0 sc1
v_mfma_f32_16x16x32_bf16 v[112:115], v[216:219], a[240:243], v[112:115]
ds_read_b128 a[204:207], v33 offset:34816
s_waitcnt lgkmcnt(8)
v_mfma_f32_16x16x32_bf16 v[148:151], v[208:211], a[212:215], v[148:151]
v_mfma_f32_16x16x32_bf16 v[84:87], v[200:203], a[212:215], v[84:87]
ds_read_b128 a[236:239], v35 offset:34816
s_waitcnt lgkmcnt(8)
v_mfma_f32_16x16x32_bf16 v[84:87], v[212:215], a[244:247], v[84:87]
buffer_load_dwordx4 v[44:47], v16, s[20:23], s97 offen offset:1024 sc1
v_mfma_f32_16x16x32_bf16 v[116:119], v[204:207], a[212:215], v[116:119]
ds_read_b128 a[192:195], v32 offset:32768
v_mfma_f32_16x16x32_bf16 v[116:119], v[216:219], a[244:247], v[116:119]
s_waitcnt lgkmcnt(8)
v_mfma_f32_16x16x32_bf16 v[152:155], v[208:211], a[216:219], v[152:155]
ds_read_b128 a[224:227], v34 offset:32768
v_mfma_f32_16x16x32_bf16 v[88:91], v[200:203], a[216:219], v[88:91]
buffer_load_dwordx4 v[48:51], v17, s[20:23], s97 offen offset:0 sc1
s_waitcnt lgkmcnt(8)
v_mfma_f32_16x16x32_bf16 v[88:91], v[212:215], a[248:251], v[88:91]
ds_read_b128 a[196:199], v32 offset:34816
v_mfma_f32_16x16x32_bf16 v[120:123], v[204:207], a[216:219], v[120:123]
buffer_load_dwordx4 v[52:55], v18, s[20:23], s97 offen offset:0 sc1
v_mfma_f32_16x16x32_bf16 v[120:123], v[216:219], a[248:251], v[120:123]
ds_read_b128 a[228:231], v34 offset:34816
s_waitcnt lgkmcnt(9)
v_mfma_f32_16x16x32_bf16 v[156:159], v[208:211], a[220:223], v[156:159]
v_mfma_f32_16x16x32_bf16 v[92:95], v[200:203], a[220:223], v[92:95]
s_waitcnt lgkmcnt(8)
v_mfma_f32_16x16x32_bf16 v[92:95], v[212:215], a[252:255], v[92:95]
buffer_load_dwordx4 v[56:59], v18, s[20:23], s97 offen offset:1024 sc1
v_mfma_f32_16x16x32_bf16 v[124:127], v[204:207], a[220:223], v[124:127]
v_mfma_f32_16x16x32_bf16 v[124:127], v[216:219], a[252:255], v[124:127]
s_waitcnt lgkmcnt(0)
s_barrier
v_mfma_f32_16x16x32_bf16 v[136:139], v[208:211], a[200:203], v[136:139]
ds_read_b128 a[208:211], v32 offset:8192
s_add_u32 m0, s94, 98304
s_bfe_u32 s91, s9, 0x40010
s_lshl_b32 s91, s91, 7
s_add_u32 s91, s91, s12
v_mfma_f32_16x16x32_bf16 v[72:75], v[200:203], a[200:203], v[72:75]
buffer_load_dwordx4 v24, s[32:35], s91 offen offset:0 lds
v_mfma_f32_16x16x32_bf16 v[72:75], v[212:215], a[232:235], v[72:75]
ds_read_b128 a[240:243], v34 offset:8192
v_mfma_f32_16x16x32_bf16 v[104:107], v[204:207], a[200:203], v[104:107]
buffer_load_dwordx4 v25, s[32:35], s91 offen offset:1024 lds
v_mfma_f32_16x16x32_bf16 v[104:107], v[216:219], a[232:235], v[104:107]
ds_read_b128 a[212:215], v32 offset:10240
v_mfma_f32_16x16x32_bf16 v[140:143], v[208:211], a[204:207], v[140:143]
buffer_load_dwordx4 v26, s[32:35], s91 offen offset:2048 lds
v_mfma_f32_16x16x32_bf16 v[76:79], v[200:203], a[204:207], v[76:79]
ds_read_b128 a[244:247], v34 offset:10240
v_mfma_f32_16x16x32_bf16 v[76:79], v[212:215], a[236:239], v[76:79]
v_mfma_f32_16x16x32_bf16 v[108:111], v[204:207], a[204:207], v[108:111]
ds_read_b128 a[216:219], v33 offset:8192
v_mfma_f32_16x16x32_bf16 v[108:111], v[216:219], a[236:239], v[108:111]
buffer_load_dwordx4 v27, s[32:35], s91 offen offset:3072 lds
v_mfma_f32_16x16x32_bf16 v[128:131], v[208:211], a[192:195], v[128:131]
ds_read_b128 a[248:251], v35 offset:8192
s_add_u32 m0, s94, 102400
s_add_u32 s91, s91, 131072
v_mfma_f32_16x16x32_bf16 v[64:67], v[200:203], a[192:195], v[64:67]
buffer_load_dwordx4 v24, s[32:35], s91 offen offset:0 lds
v_mfma_f32_16x16x32_bf16 v[64:67], v[212:215], a[224:227], v[64:67]
ds_read_b128 a[220:223], v33 offset:10240
v_mfma_f32_16x16x32_bf16 v[96:99], v[204:207], a[192:195], v[96:99]
v_mfma_f32_16x16x32_bf16 v[96:99], v[216:219], a[224:227], v[96:99]
ds_read_b128 a[252:255], v35 offset:10240
v_mfma_f32_16x16x32_bf16 v[132:135], v[208:211], a[196:199], v[132:135]
buffer_load_dwordx4 v25, s[32:35], s91 offen offset:1024 lds
v_mfma_f32_16x16x32_bf16 v[68:71], v[200:203], a[196:199], v[68:71]
v_mfma_f32_16x16x32_bf16 v[68:71], v[212:215], a[228:231], v[68:71]
buffer_load_dwordx4 v26, s[32:35], s91 offen offset:2048 lds
v_mfma_f32_16x16x32_bf16 v[100:103], v[204:207], a[196:199], v[100:103]
v_mfma_f32_16x16x32_bf16 v[100:103], v[216:219], a[228:231], v[100:103]
s_waitcnt vmcnt(22) lgkmcnt(7)
v_mfma_f32_16x16x32_bf16 v[144:147], v[228:231], a[208:211], v[144:147]
ds_read_b128 a[200:203], v33 offset:0
v_mfma_f32_16x16x32_bf16 v[80:83], v[220:223], a[208:211], v[80:83]
buffer_load_dwordx4 v27, s[32:35], s91 offen offset:3072 lds
s_waitcnt vmcnt(22) lgkmcnt(7)
v_mfma_f32_16x16x32_bf16 v[80:83], v[232:235], a[240:243], v[80:83]
ds_read_b128 a[232:235], v35 offset:0
s_bfe_u32 s97, s9, 0x40008
s_mul_i32 s97, s97, 10240
s_add_u32 s97, s97, s96
v_mfma_f32_16x16x32_bf16 v[112:115], v[224:227], a[208:211], v[112:115]
buffer_load_dwordx4 v[160:163], v16, s[20:23], s97 offen offset:0 sc1
s_waitcnt vmcnt(22)
v_mfma_f32_16x16x32_bf16 v[112:115], v[236:239], a[240:243], v[112:115]
ds_read_b128 a[204:207], v33 offset:2048
s_waitcnt lgkmcnt(8)
v_mfma_f32_16x16x32_bf16 v[148:151], v[228:231], a[212:215], v[148:151]
v_mfma_f32_16x16x32_bf16 v[84:87], v[220:223], a[212:215], v[84:87]
ds_read_b128 a[236:239], v35 offset:2048
s_waitcnt lgkmcnt(8)
v_mfma_f32_16x16x32_bf16 v[84:87], v[232:235], a[244:247], v[84:87]
buffer_load_dwordx4 v[164:167], v16, s[20:23], s97 offen offset:1024 sc1
v_mfma_f32_16x16x32_bf16 v[116:119], v[224:227], a[212:215], v[116:119]
ds_read_b128 a[192:195], v32 offset:0
v_mfma_f32_16x16x32_bf16 v[116:119], v[236:239], a[244:247], v[116:119]
s_waitcnt lgkmcnt(8)
v_mfma_f32_16x16x32_bf16 v[152:155], v[228:231], a[216:219], v[152:155]
ds_read_b128 a[224:227], v34 offset:0
v_mfma_f32_16x16x32_bf16 v[88:91], v[220:223], a[216:219], v[88:91]
buffer_load_dwordx4 v[168:171], v17, s[20:23], s97 offen offset:0 sc1
s_waitcnt lgkmcnt(8)
v_mfma_f32_16x16x32_bf16 v[88:91], v[232:235], a[248:251], v[88:91]
ds_read_b128 a[196:199], v32 offset:2048
v_mfma_f32_16x16x32_bf16 v[120:123], v[224:227], a[216:219], v[120:123]
buffer_load_dwordx4 v[172:175], v18, s[20:23], s97 offen offset:0 sc1
v_mfma_f32_16x16x32_bf16 v[120:123], v[236:239], a[248:251], v[120:123]
ds_read_b128 a[228:231], v34 offset:2048
s_waitcnt lgkmcnt(9)
v_mfma_f32_16x16x32_bf16 v[156:159], v[228:231], a[220:223], v[156:159]
v_mfma_f32_16x16x32_bf16 v[92:95], v[220:223], a[220:223], v[92:95]
s_waitcnt lgkmcnt(8)
v_mfma_f32_16x16x32_bf16 v[92:95], v[232:235], a[252:255], v[92:95]
buffer_load_dwordx4 v[176:179], v18, s[20:23], s97 offen offset:1024 sc1
v_mfma_f32_16x16x32_bf16 v[124:127], v[224:227], a[220:223], v[124:127]
)A"
R"A(v_mfma_f32_16x16x32_bf16 v[124:127], v[236:239], a[252:255], v[124:127]
s_waitcnt lgkmcnt(0)
s_barrier
v_mfma_f32_16x16x32_bf16 v[136:139], v[228:231], a[200:203], v[136:139]
ds_read_b128 a[208:211], v28 offset:40960
s_add_u32 m0, s94, 65536
s_bfe_u32 s91, s9, 0x40014
s_lshl_b32 s91, s91, 7
s_add_u32 s91, s91, s12
v_mfma_f32_16x16x32_bf16 v[72:75], v[220:223], a[200:203], v[72:75]
buffer_load_dwordx4 v24, s[32:35], s91 offen offset:0 lds
v_mfma_f32_16x16x32_bf16 v[72:75], v[232:235], a[232:235], v[72:75]
ds_read_b128 a[240:243], v30 offset:40960
v_mfma_f32_16x16x32_bf16 v[104:107], v[224:227], a[200:203], v[104:107]
buffer_load_dwordx4 v25, s[32:35], s91 offen offset:1024 lds
v_mfma_f32_16x16x32_bf16 v[104:107], v[236:239], a[232:235], v[104:107]
ds_read_b128 a[212:215], v28 offset:43008
v_mfma_f32_16x16x32_bf16 v[140:143], v[228:231], a[204:207], v[140:143]
buffer_load_dwordx4 v26, s[32:35], s91 offen offset:2048 lds
v_mfma_f32_16x16x32_bf16 v[76:79], v[220:223], a[204:207], v[76:79]
ds_read_b128 a[244:247], v30 offset:43008
v_mfma_f32_16x16x32_bf16 v[76:79], v[232:235], a[236:239], v[76:79]
v_mfma_f32_16x16x32_bf16 v[108:111], v[224:227], a[204:207], v[108:111]
ds_read_b128 a[216:219], v29 offset:40960
v_mfma_f32_16x16x32_bf16 v[108:111], v[236:239], a[236:239], v[108:111]
buffer_load_dwordx4 v27, s[32:35], s91 offen offset:3072 lds
v_mfma_f32_16x16x32_bf16 v[128:131], v[228:231], a[192:195], v[128:131]
ds_read_b128 a[248:251], v31 offset:40960
s_add_u32 m0, s94, 69632
s_add_u32 s91, s91, 131072
v_mfma_f32_16x16x32_bf16 v[64:67], v[220:223], a[192:195], v[64:67]
buffer_load_dwordx4 v24, s[32:35], s91 offen offset:0 lds
v_mfma_f32_16x16x32_bf16 v[64:67], v[232:235], a[224:227], v[64:67]
ds_read_b128 a[220:223], v29 offset:43008
v_mfma_f32_16x16x32_bf16 v[96:99], v[224:227], a[192:195], v[96:99]
v_mfma_f32_16x16x32_bf16 v[96:99], v[236:239], a[224:227], v[96:99]
ds_read_b128 a[252:255], v31 offset:43008
v_mfma_f32_16x16x32_bf16 v[132:135], v[228:231], a[196:199], v[132:135]
buffer_load_dwordx4 v25, s[32:35], s91 offen offset:1024 lds
v_mfma_f32_16x16x32_bf16 v[68:71], v[220:223], a[196:199], v[68:71]
v_mfma_f32_16x16x32_bf16 v[68:71], v[232:235], a[228:231], v[68:71]
buffer_load_dwordx4 v26, s[32:35], s91 offen offset:2048 lds
v_mfma_f32_16x16x32_bf16 v[100:103], v[224:227], a[196:199], v[100:103]
v_mfma_f32_16x16x32_bf16 v[100:103], v[236:239], a[228:231], v[100:103]
s_waitcnt vmcnt(22) lgkmcnt(7)
v_mfma_f32_16x16x32_bf16 v[144:147], v[48:51], a[208:211], v[144:147]
ds_read_b128 a[200:203], v29 offset:32768
v_mfma_f32_16x16x32_bf16 v[80:83], v[40:43], a[208:211], v[80:83]
buffer_load_dwordx4 v27, s[32:35], s91 offen offset:3072 lds
s_waitcnt vmcnt(22) lgkmcnt(7)
v_mfma_f32_16x16x32_bf16 v[80:83], v[52:55], a[240:243], v[80:83]
ds_read_b128 a[232:235], v31 offset:32768
s_bfe_u32 s97, s9, 0x4000c
s_mul_i32 s97, s97, 10240
s_add_u32 s97, s97, s96
v_mfma_f32_16x16x32_bf16 v[112:115], v[44:47], a[208:211], v[112:115]
buffer_load_dwordx4 v[180:183], v16, s[20:23], s97 offen offset:0 sc1
s_waitcnt vmcnt(22)
v_mfma_f32_16x16x32_bf16 v[112:115], v[56:59], a[240:243], v[112:115]
ds_read_b128 a[204:207], v29 offset:34816
s_waitcnt lgkmcnt(8)
v_mfma_f32_16x16x32_bf16 v[148:151], v[48:51], a[212:215], v[148:151]
v_mfma_f32_16x16x32_bf16 v[84:87], v[40:43], a[212:215], v[84:87]
ds_read_b128 a[236:239], v31 offset:34816
s_waitcnt lgkmcnt(8)
v_mfma_f32_16x16x32_bf16 v[84:87], v[52:55], a[244:247], v[84:87]
buffer_load_dwordx4 v[184:187], v16, s[20:23], s97 offen offset:1024 sc1
v_mfma_f32_16x16x32_bf16 v[116:119], v[44:47], a[212:215], v[116:119]
ds_read_b128 a[192:195], v28 offset:32768
v_mfma_f32_16x16x32_bf16 v[116:119], v[56:59], a[244:247], v[116:119]
s_waitcnt lgkmcnt(8)
v_mfma_f32_16x16x32_bf16 v[152:155], v[48:51], a[216:219], v[152:155]
ds_read_b128 a[224:227], v30 offset:32768
v_mfma_f32_16x16x32_bf16 v[88:91], v[40:43], a[216:219], v[88:91]
buffer_load_dwordx4 v[188:191], v17, s[20:23], s97 offen offset:0 sc1
s_waitcnt lgkmcnt(8)
v_mfma_f32_16x16x32_bf16 v[88:91], v[52:55], a[248:251], v[88:91]
ds_read_b128 a[196:199], v28 offset:34816
v_mfma_f32_16x16x32_bf16 v[120:123], v[44:47], a[216:219], v[120:123]
buffer_load_dwordx4 v[192:195], v18, s[20:23], s97 offen offset:0 sc1
v_mfma_f32_16x16x32_bf16 v[120:123], v[56:59], a[248:251], v[120:123]
ds_read_b128 a[228:231], v30 offset:34816
s_waitcnt lgkmcnt(9)
v_mfma_f32_16x16x32_bf16 v[156:159], v[48:51], a[220:223], v[156:159]
v_mfma_f32_16x16x32_bf16 v[92:95], v[40:43], a[220:223], v[92:95]
s_waitcnt lgkmcnt(8)
v_mfma_f32_16x16x32_bf16 v[92:95], v[52:55], a[252:255], v[92:95]
buffer_load_dwordx4 v[196:199], v18, s[20:23], s97 offen offset:1024 sc1
v_mfma_f32_16x16x32_bf16 v[124:127], v[44:47], a[220:223], v[124:127]
v_mfma_f32_16x16x32_bf16 v[124:127], v[56:59], a[252:255], v[124:127]
s_waitcnt lgkmcnt(0)
s_barrier
v_mfma_f32_16x16x32_bf16 v[136:139], v[48:51], a[200:203], v[136:139]
ds_read_b128 a[208:211], v28 offset:8192
s_add_u32 m0, s94, 32768
s_bfe_u32 s91, s9, 0x40018
s_lshl_b32 s91, s91, 7
s_add_u32 s91, s91, s12
v_mfma_f32_16x16x32_bf16 v[72:75], v[40:43], a[200:203], v[72:75]
buffer_load_dwordx4 v24, s[32:35], s91 offen offset:0 lds
v_mfma_f32_16x16x32_bf16 v[72:75], v[52:55], a[232:235], v[72:75]
ds_read_b128 a[240:243], v30 offset:8192
v_mfma_f32_16x16x32_bf16 v[104:107], v[44:47], a[200:203], v[104:107]
buffer_load_dwordx4 v25, s[32:35], s91 offen offset:1024 lds
v_mfma_f32_16x16x32_bf16 v[104:107], v[56:59], a[232:235], v[104:107]
ds_read_b128 a[212:215], v28 offset:10240
v_mfma_f32_16x16x32_bf16 v[140:143], v[48:51], a[204:207], v[140:143]
buffer_load_dwordx4 v26, s[32:35], s91 offen offset:2048 lds
v_mfma_f32_16x16x32_bf16 v[76:79], v[40:43], a[204:207], v[76:79]
ds_read_b128 a[244:247], v30 offset:10240
v_mfma_f32_16x16x32_bf16 v[76:79], v[52:55], a[236:239], v[76:79]
v_mfma_f32_16x16x32_bf16 v[108:111], v[44:47], a[204:207], v[108:111]
ds_read_b128 a[216:219], v29 offset:8192
v_mfma_f32_16x16x32_bf16 v[108:111], v[56:59], a[236:239], v[108:111]
buffer_load_dwordx4 v27, s[32:35], s91 offen offset:3072 lds
v_mfma_f32_16x16x32_bf16 v[128:131], v[48:51], a[192:195], v[128:131]
ds_read_b128 a[248:251], v31 offset:8192
s_add_u32 m0, s94, 36864
s_add_u32 s91, s91, 131072
v_mfma_f32_16x16x32_bf16 v[64:67], v[40:43], a[192:195], v[64:67]
buffer_load_dwordx4 v24, s[32:35], s91 offen offset:0 lds
v_mfma_f32_16x16x32_bf16 v[64:67], v[52:55], a[224:227], v[64:67]
ds_read_b128 a[220:223], v29 offset:10240
v_mfma_f32_16x16x32_bf16 v[96:99], v[44:47], a[192:195], v[96:99]
v_mfma_f32_16x16x32_bf16 v[96:99], v[56:59], a[224:227], v[96:99]
ds_read_b128 a[252:255], v31 offset:10240
v_mfma_f32_16x16x32_bf16 v[132:135], v[48:51], a[196:199], v[132:135]
buffer_load_dwordx4 v25, s[32:35], s91 offen offset:1024 lds
v_mfma_f32_16x16x32_bf16 v[68:71], v[40:43], a[196:199], v[68:71]
v_mfma_f32_16x16x32_bf16 v[68:71], v[52:55], a[228:231], v[68:71]
buffer_load_dwordx4 v26, s[32:35], s91 offen offset:2048 lds
v_mfma_f32_16x16x32_bf16 v[100:103], v[44:47], a[196:199], v[100:103]
v_mfma_f32_16x16x32_bf16 v[100:103], v[56:59], a[228:231], v[100:103]
s_waitcnt vmcnt(22) lgkmcnt(7)
v_mfma_f32_16x16x32_bf16 v[144:147], v[168:171], a[208:211], v[144:147]
ds_read_b128 a[200:203], v29 offset:0
v_mfma_f32_16x16x32_bf16 v[80:83], v[160:163], a[208:211], v[80:83]
buffer_load_dwordx4 v27, s[32:35], s91 offen offset:3072 lds
s_waitcnt vmcnt(22) lgkmcnt(7)
v_mfma_f32_16x16x32_bf16 v[80:83], v[172:175], a[240:243], v[80:83]
ds_read_b128 a[232:235], v31 offset:0
s_bfe_u32 s97, s9, 0x40010
s_mul_i32 s97, s97, 10240
s_add_u32 s97, s97, s96
v_mfma_f32_16x16x32_bf16 v[112:115], v[164:167], a[208:211], v[112:115]
buffer_load_dwordx4 v[200:203], v16, s[20:23], s97 offen offset:0 sc1
s_waitcnt vmcnt(22)
v_mfma_f32_16x16x32_bf16 v[112:115], v[176:179], a[240:243], v[112:115]
ds_read_b128 a[204:207], v29 offset:2048
s_waitcnt lgkmcnt(8)
v_mfma_f32_16x16x32_bf16 v[148:151], v[168:171], a[212:215], v[148:151]
v_mfma_f32_16x16x32_bf16 v[84:87], v[160:163], a[212:215], v[84:87]
)A"
R"A(ds_read_b128 a[236:239], v31 offset:2048
s_waitcnt lgkmcnt(8)
v_mfma_f32_16x16x32_bf16 v[84:87], v[172:175], a[244:247], v[84:87]
buffer_load_dwordx4 v[204:207], v16, s[20:23], s97 offen offset:1024 sc1
v_mfma_f32_16x16x32_bf16 v[116:119], v[164:167], a[212:215], v[116:119]
ds_read_b128 a[192:195], v28 offset:0
v_mfma_f32_16x16x32_bf16 v[116:119], v[176:179], a[244:247], v[116:119]
s_waitcnt lgkmcnt(8)
v_mfma_f32_16x16x32_bf16 v[152:155], v[168:171], a[216:219], v[152:155]
ds_read_b128 a[224:227], v30 offset:0
v_mfma_f32_16x16x32_bf16 v[88:91], v[160:163], a[216:219], v[88:91]
buffer_load_dwordx4 v[208:211], v17, s[20:23], s97 offen offset:0 sc1
s_waitcnt lgkmcnt(8)
v_mfma_f32_16x16x32_bf16 v[88:91], v[172:175], a[248:251], v[88:91]
ds_read_b128 a[196:199], v28 offset:2048
v_mfma_f32_16x16x32_bf16 v[120:123], v[164:167], a[216:219], v[120:123]
buffer_load_dwordx4 v[212:215], v18, s[20:23], s97 offen offset:0 sc1
v_mfma_f32_16x16x32_bf16 v[120:123], v[176:179], a[248:251], v[120:123]
ds_read_b128 a[228:231], v30 offset:2048
s_waitcnt lgkmcnt(9)
v_mfma_f32_16x16x32_bf16 v[156:159], v[168:171], a[220:223], v[156:159]
v_mfma_f32_16x16x32_bf16 v[92:95], v[160:163], a[220:223], v[92:95]
s_waitcnt lgkmcnt(8)
v_mfma_f32_16x16x32_bf16 v[92:95], v[172:175], a[252:255], v[92:95]
buffer_load_dwordx4 v[216:219], v18, s[20:23], s97 offen offset:1024 sc1
v_mfma_f32_16x16x32_bf16 v[124:127], v[164:167], a[220:223], v[124:127]
v_mfma_f32_16x16x32_bf16 v[124:127], v[176:179], a[252:255], v[124:127]
s_waitcnt lgkmcnt(0)
s_barrier
v_mfma_f32_16x16x32_bf16 v[136:139], v[168:171], a[200:203], v[136:139]
ds_read_b128 a[208:211], v36 offset:8192
s_add_u32 m0, s94, 0
s_bfe_u32 s91, s9, 0x4001c
s_lshl_b32 s91, s91, 7
s_add_u32 s91, s91, s12
v_mfma_f32_16x16x32_bf16 v[72:75], v[160:163], a[200:203], v[72:75]
buffer_load_dwordx4 v24, s[32:35], s91 offen offset:0 lds
v_mfma_f32_16x16x32_bf16 v[72:75], v[172:175], a[232:235], v[72:75]
ds_read_b128 a[240:243], v38 offset:8192
v_mfma_f32_16x16x32_bf16 v[104:107], v[164:167], a[200:203], v[104:107]
buffer_load_dwordx4 v25, s[32:35], s91 offen offset:1024 lds
v_mfma_f32_16x16x32_bf16 v[104:107], v[176:179], a[232:235], v[104:107]
ds_read_b128 a[212:215], v36 offset:10240
v_mfma_f32_16x16x32_bf16 v[140:143], v[168:171], a[204:207], v[140:143]
buffer_load_dwordx4 v26, s[32:35], s91 offen offset:2048 lds
v_mfma_f32_16x16x32_bf16 v[76:79], v[160:163], a[204:207], v[76:79]
ds_read_b128 a[244:247], v38 offset:10240
v_mfma_f32_16x16x32_bf16 v[76:79], v[172:175], a[236:239], v[76:79]
buffer_load_dwordx4 v27, s[32:35], s91 offen offset:3072 lds
v_mfma_f32_16x16x32_bf16 v[108:111], v[164:167], a[204:207], v[108:111]
ds_read_b128 a[216:219], v37 offset:8192
s_add_u32 m0, s94, 4096
s_add_u32 s91, s91, 131072
v_mfma_f32_16x16x32_bf16 v[108:111], v[176:179], a[236:239], v[108:111]
buffer_load_dwordx4 v24, s[32:35], s91 offen offset:0 lds
v_mfma_f32_16x16x32_bf16 v[128:131], v[168:171], a[192:195], v[128:131]
ds_read_b128 a[248:251], v39 offset:8192
v_mfma_f32_16x16x32_bf16 v[64:67], v[160:163], a[192:195], v[64:67]
buffer_load_dwordx4 v25, s[32:35], s91 offen offset:1024 lds
v_mfma_f32_16x16x32_bf16 v[64:67], v[172:175], a[224:227], v[64:67]
ds_read_b128 a[220:223], v37 offset:10240
v_mfma_f32_16x16x32_bf16 v[96:99], v[164:167], a[192:195], v[96:99]
buffer_load_dwordx4 v26, s[32:35], s91 offen offset:2048 lds
v_mfma_f32_16x16x32_bf16 v[96:99], v[176:179], a[224:227], v[96:99]
ds_read_b128 a[252:255], v39 offset:10240
v_mfma_f32_16x16x32_bf16 v[132:135], v[168:171], a[196:199], v[132:135]
buffer_load_dwordx4 v27, s[32:35], s91 offen offset:3072 lds
v_mfma_f32_16x16x32_bf16 v[68:71], v[160:163], a[196:199], v[68:71]
s_bfe_u32 s97, s9, 0x40014
s_mul_i32 s97, s97, 10240
s_add_u32 s97, s97, s96
v_mfma_f32_16x16x32_bf16 v[68:71], v[172:175], a[228:231], v[68:71]
buffer_load_dwordx4 v[220:223], v16, s[20:23], s97 offen offset:0 sc1
v_mfma_f32_16x16x32_bf16 v[100:103], v[164:167], a[196:199], v[100:103]
v_mfma_f32_16x16x32_bf16 v[100:103], v[176:179], a[228:231], v[100:103]
s_waitcnt vmcnt(24) lgkmcnt(7)
v_mfma_f32_16x16x32_bf16 v[144:147], v[188:191], a[208:211], v[144:147]
ds_read_b128 a[200:203], v37 offset:0
v_mfma_f32_16x16x32_bf16 v[80:83], v[180:183], a[208:211], v[80:83]
buffer_load_dwordx4 v[224:227], v16, s[20:23], s97 offen offset:1024 sc1
s_waitcnt vmcnt(24) lgkmcnt(7)
v_mfma_f32_16x16x32_bf16 v[80:83], v[192:195], a[240:243], v[80:83]
ds_read_b128 a[232:235], v39 offset:0
v_mfma_f32_16x16x32_bf16 v[112:115], v[184:187], a[208:211], v[112:115]
buffer_load_dwordx4 v[228:231], v17, s[20:23], s97 offen offset:0 sc1
s_waitcnt vmcnt(24)
v_mfma_f32_16x16x32_bf16 v[112:115], v[196:199], a[240:243], v[112:115]
ds_read_b128 a[204:207], v37 offset:2048
s_waitcnt lgkmcnt(8)
v_mfma_f32_16x16x32_bf16 v[148:151], v[188:191], a[212:215], v[148:151]
buffer_load_dwordx4 v[232:235], v18, s[20:23], s97 offen offset:0 sc1
v_mfma_f32_16x16x32_bf16 v[84:87], v[180:183], a[212:215], v[84:87]
ds_read_b128 a[236:239], v39 offset:2048
s_waitcnt lgkmcnt(8)
v_mfma_f32_16x16x32_bf16 v[84:87], v[192:195], a[244:247], v[84:87]
buffer_load_dwordx4 v[236:239], v18, s[20:23], s97 offen offset:1024 sc1
v_mfma_f32_16x16x32_bf16 v[116:119], v[184:187], a[212:215], v[116:119]
ds_read_b128 a[192:195], v36 offset:0
s_bfe_u32 s97, s9, 0x40018
s_mul_i32 s97, s97, 10240
s_add_u32 s97, s97, s96
v_mfma_f32_16x16x32_bf16 v[116:119], v[196:199], a[244:247], v[116:119]
buffer_load_dwordx4 v[40:43], v16, s[20:23], s97 offen offset:0 sc1
s_waitcnt lgkmcnt(8)
v_mfma_f32_16x16x32_bf16 v[152:155], v[188:191], a[216:219], v[152:155]
ds_read_b128 a[224:227], v38 offset:0
v_mfma_f32_16x16x32_bf16 v[88:91], v[180:183], a[216:219], v[88:91]
buffer_load_dwordx4 v[44:47], v16, s[20:23], s97 offen offset:1024 sc1
s_waitcnt lgkmcnt(8)
v_mfma_f32_16x16x32_bf16 v[88:91], v[192:195], a[248:251], v[88:91]
ds_read_b128 a[196:199], v36 offset:2048
v_mfma_f32_16x16x32_bf16 v[120:123], v[184:187], a[216:219], v[120:123]
buffer_load_dwordx4 v[48:51], v17, s[20:23], s97 offen offset:0 sc1
v_mfma_f32_16x16x32_bf16 v[120:123], v[196:199], a[248:251], v[120:123]
ds_read_b128 a[228:231], v38 offset:2048
s_waitcnt lgkmcnt(9)
v_mfma_f32_16x16x32_bf16 v[156:159], v[188:191], a[220:223], v[156:159]
buffer_load_dwordx4 v[52:55], v18, s[20:23], s97 offen offset:0 sc1
v_mfma_f32_16x16x32_bf16 v[92:95], v[180:183], a[220:223], v[92:95]
s_waitcnt lgkmcnt(8)
v_mfma_f32_16x16x32_bf16 v[92:95], v[192:195], a[252:255], v[92:95]
buffer_load_dwordx4 v[56:59], v18, s[20:23], s97 offen offset:1024 sc1
v_mfma_f32_16x16x32_bf16 v[124:127], v[184:187], a[220:223], v[124:127]
v_mfma_f32_16x16x32_bf16 v[124:127], v[196:199], a[252:255], v[124:127]
s_waitcnt lgkmcnt(0)
s_barrier
v_mfma_f32_16x16x32_bf16 v[136:139], v[188:191], a[200:203], v[136:139]
ds_read_b128 a[208:211], v32 offset:40960
s_bfe_u32 s97, s9, 0x4001c
s_mul_i32 s97, s97, 10240
s_add_u32 s97, s97, s96
v_mfma_f32_16x16x32_bf16 v[72:75], v[180:183], a[200:203], v[72:75]
buffer_load_dwordx4 v[160:163], v16, s[20:23], s97 offen offset:0 sc1
v_mfma_f32_16x16x32_bf16 v[72:75], v[192:195], a[232:235], v[72:75]
ds_read_b128 a[240:243], v34 offset:40960
v_mfma_f32_16x16x32_bf16 v[104:107], v[184:187], a[200:203], v[104:107]
buffer_load_dwordx4 v[164:167], v16, s[20:23], s97 offen offset:1024 sc1
v_mfma_f32_16x16x32_bf16 v[104:107], v[196:199], a[232:235], v[104:107]
ds_read_b128 a[212:215], v32 offset:43008
v_mfma_f32_16x16x32_bf16 v[140:143], v[188:191], a[204:207], v[140:143]
v_mfma_f32_16x16x32_bf16 v[76:79], v[180:183], a[204:207], v[76:79]
ds_read_b128 a[244:247], v34 offset:43008
v_mfma_f32_16x16x32_bf16 v[76:79], v[192:195], a[236:239], v[76:79]
buffer_load_dwordx4 v[168:171], v17, s[20:23], s97 offen offset:0 sc1
v_mfma_f32_16x16x32_bf16 v[108:111], v[184:187], a[204:207], v[108:111]
ds_read_b128 a[216:219], v33 offset:40960
v_mfma_f32_16x16x32_bf16 v[108:111], v[196:199], a[236:239], v[108:111]
v_mfma_f32_16x16x32_bf16 v[128:131], v[188:191], a[192:195], v[128:131]
ds_read_b128 a[248:251], v35 offset:40960
)A"
R"A(v_mfma_f32_16x16x32_bf16 v[64:67], v[180:183], a[192:195], v[64:67]
buffer_load_dwordx4 v[172:175], v18, s[20:23], s97 offen offset:0 sc1
v_mfma_f32_16x16x32_bf16 v[64:67], v[192:195], a[224:227], v[64:67]
ds_read_b128 a[220:223], v33 offset:43008
v_mfma_f32_16x16x32_bf16 v[96:99], v[184:187], a[192:195], v[96:99]
buffer_load_dwordx4 v[176:179], v18, s[20:23], s97 offen offset:1024 sc1
v_mfma_f32_16x16x32_bf16 v[96:99], v[196:199], a[224:227], v[96:99]
ds_read_b128 a[252:255], v35 offset:43008
v_mfma_f32_16x16x32_bf16 v[132:135], v[188:191], a[196:199], v[132:135]
v_mfma_f32_16x16x32_bf16 v[68:71], v[180:183], a[196:199], v[68:71]
v_mfma_f32_16x16x32_bf16 v[68:71], v[192:195], a[228:231], v[68:71]
global_load_dword v252, v253, s[10:11]
v_mfma_f32_16x16x32_bf16 v[100:103], v[184:187], a[196:199], v[100:103]
v_mfma_f32_16x16x32_bf16 v[100:103], v[196:199], a[228:231], v[100:103]
s_waitcnt vmcnt(26) lgkmcnt(7)
v_mfma_f32_16x16x32_bf16 v[144:147], v[208:211], a[208:211], v[144:147]
ds_read_b128 a[200:203], v33 offset:32768
s_add_u32 s91, s30, 0
v_mfma_f32_16x16x32_bf16 v[80:83], v[200:203], a[208:211], v[80:83]
buffer_load_dwordx4 a[0:3], v1, s[20:23], s91 offen offset:0 sc1
s_waitcnt vmcnt(26) lgkmcnt(7)
v_mfma_f32_16x16x32_bf16 v[80:83], v[212:215], a[240:243], v[80:83]
ds_read_b128 a[232:235], v35 offset:32768
v_mfma_f32_16x16x32_bf16 v[112:115], v[204:207], a[208:211], v[112:115]
buffer_load_dwordx4 a[4:7], v1, s[20:23], s91 offen offset:1024 sc1
s_waitcnt vmcnt(26)
v_mfma_f32_16x16x32_bf16 v[112:115], v[216:219], a[240:243], v[112:115]
ds_read_b128 a[204:207], v33 offset:34816
s_waitcnt lgkmcnt(8)
v_mfma_f32_16x16x32_bf16 v[148:151], v[208:211], a[212:215], v[148:151]
v_mfma_f32_16x16x32_bf16 v[84:87], v[200:203], a[212:215], v[84:87]
ds_read_b128 a[236:239], v35 offset:34816
s_waitcnt lgkmcnt(8)
v_mfma_f32_16x16x32_bf16 v[84:87], v[212:215], a[244:247], v[84:87]
buffer_load_dwordx4 a[8:11], v1, s[20:23], s91 offen offset:2048 sc1
v_mfma_f32_16x16x32_bf16 v[116:119], v[204:207], a[212:215], v[116:119]
ds_read_b128 a[192:195], v32 offset:32768
v_mfma_f32_16x16x32_bf16 v[116:119], v[216:219], a[244:247], v[116:119]
s_waitcnt lgkmcnt(8)
v_mfma_f32_16x16x32_bf16 v[152:155], v[208:211], a[216:219], v[152:155]
ds_read_b128 a[224:227], v34 offset:32768
v_mfma_f32_16x16x32_bf16 v[88:91], v[200:203], a[216:219], v[88:91]
buffer_load_dwordx4 a[12:15], v1, s[20:23], s91 offen offset:3072 sc1
s_waitcnt lgkmcnt(8)
v_mfma_f32_16x16x32_bf16 v[88:91], v[212:215], a[248:251], v[88:91]
ds_read_b128 a[196:199], v32 offset:34816
s_add_u32 s91, s30, 4096
v_mfma_f32_16x16x32_bf16 v[120:123], v[204:207], a[216:219], v[120:123]
buffer_load_dwordx4 a[16:19], v1, s[20:23], s91 offen offset:0 sc1
v_mfma_f32_16x16x32_bf16 v[120:123], v[216:219], a[248:251], v[120:123]
ds_read_b128 a[228:231], v34 offset:34816
s_waitcnt lgkmcnt(9)
v_mfma_f32_16x16x32_bf16 v[156:159], v[208:211], a[220:223], v[156:159]
v_mfma_f32_16x16x32_bf16 v[92:95], v[200:203], a[220:223], v[92:95]
s_waitcnt lgkmcnt(8)
v_mfma_f32_16x16x32_bf16 v[92:95], v[212:215], a[252:255], v[92:95]
buffer_load_dwordx4 a[20:23], v1, s[20:23], s91 offen offset:1024 sc1
v_mfma_f32_16x16x32_bf16 v[124:127], v[204:207], a[220:223], v[124:127]
v_mfma_f32_16x16x32_bf16 v[124:127], v[216:219], a[252:255], v[124:127]
s_waitcnt lgkmcnt(0)
s_barrier
v_mfma_f32_16x16x32_bf16 v[136:139], v[208:211], a[200:203], v[136:139]
ds_read_b128 a[208:211], v32 offset:8192
v_mfma_f32_16x16x32_bf16 v[72:75], v[200:203], a[200:203], v[72:75]
buffer_load_dwordx4 a[24:27], v1, s[20:23], s91 offen offset:2048 sc1
v_mfma_f32_16x16x32_bf16 v[72:75], v[212:215], a[232:235], v[72:75]
ds_read_b128 a[240:243], v34 offset:8192
v_mfma_f32_16x16x32_bf16 v[104:107], v[204:207], a[200:203], v[104:107]
buffer_load_dwordx4 a[28:31], v1, s[20:23], s91 offen offset:3072 sc1
v_mfma_f32_16x16x32_bf16 v[104:107], v[216:219], a[232:235], v[104:107]
ds_read_b128 a[212:215], v32 offset:10240
v_mfma_f32_16x16x32_bf16 v[140:143], v[208:211], a[204:207], v[140:143]
v_mfma_f32_16x16x32_bf16 v[76:79], v[200:203], a[204:207], v[76:79]
ds_read_b128 a[244:247], v34 offset:10240
s_add_u32 s91, s30, 8192
v_mfma_f32_16x16x32_bf16 v[76:79], v[212:215], a[236:239], v[76:79]
buffer_load_dwordx4 a[32:35], v1, s[20:23], s91 offen offset:0 sc1
v_mfma_f32_16x16x32_bf16 v[108:111], v[204:207], a[204:207], v[108:111]
ds_read_b128 a[216:219], v33 offset:8192
v_mfma_f32_16x16x32_bf16 v[108:111], v[216:219], a[236:239], v[108:111]
v_mfma_f32_16x16x32_bf16 v[128:131], v[208:211], a[192:195], v[128:131]
ds_read_b128 a[248:251], v35 offset:8192
v_mfma_f32_16x16x32_bf16 v[64:67], v[200:203], a[192:195], v[64:67]
buffer_load_dwordx4 a[36:39], v1, s[20:23], s91 offen offset:1024 sc1
v_mfma_f32_16x16x32_bf16 v[64:67], v[212:215], a[224:227], v[64:67]
ds_read_b128 a[220:223], v33 offset:10240
v_mfma_f32_16x16x32_bf16 v[96:99], v[204:207], a[192:195], v[96:99]
buffer_load_dwordx4 a[40:43], v1, s[20:23], s91 offen offset:2048 sc1
v_mfma_f32_16x16x32_bf16 v[96:99], v[216:219], a[224:227], v[96:99]
ds_read_b128 a[252:255], v35 offset:10240
v_mfma_f32_16x16x32_bf16 v[132:135], v[208:211], a[196:199], v[132:135]
v_mfma_f32_16x16x32_bf16 v[68:71], v[200:203], a[196:199], v[68:71]
v_mfma_f32_16x16x32_bf16 v[68:71], v[212:215], a[228:231], v[68:71]
buffer_load_dwordx4 a[44:47], v1, s[20:23], s91 offen offset:3072 sc1
v_mfma_f32_16x16x32_bf16 v[100:103], v[204:207], a[196:199], v[100:103]
v_mfma_f32_16x16x32_bf16 v[100:103], v[216:219], a[228:231], v[100:103]
s_waitcnt vmcnt(25) lgkmcnt(7)
v_mfma_f32_16x16x32_bf16 v[144:147], v[228:231], a[208:211], v[144:147]
ds_read_b128 a[200:203], v33 offset:0
s_add_u32 s91, s30, 12288
v_mfma_f32_16x16x32_bf16 v[80:83], v[220:223], a[208:211], v[80:83]
buffer_load_dwordx4 a[48:51], v1, s[20:23], s91 offen offset:0 sc1
s_waitcnt vmcnt(25) lgkmcnt(7)
v_mfma_f32_16x16x32_bf16 v[80:83], v[232:235], a[240:243], v[80:83]
ds_read_b128 a[232:235], v35 offset:0
v_mfma_f32_16x16x32_bf16 v[112:115], v[224:227], a[208:211], v[112:115]
s_waitcnt vmcnt(24)
v_mfma_f32_16x16x32_bf16 v[112:115], v[236:239], a[240:243], v[112:115]
ds_read_b128 a[204:207], v33 offset:2048
s_waitcnt lgkmcnt(8)
v_mfma_f32_16x16x32_bf16 v[148:151], v[228:231], a[212:215], v[148:151]
buffer_load_dwordx4 a[52:55], v1, s[20:23], s91 offen offset:1024 sc1
v_mfma_f32_16x16x32_bf16 v[84:87], v[220:223], a[212:215], v[84:87]
ds_read_b128 a[236:239], v35 offset:2048
s_waitcnt lgkmcnt(8)
v_mfma_f32_16x16x32_bf16 v[84:87], v[232:235], a[244:247], v[84:87]
v_mfma_f32_16x16x32_bf16 v[116:119], v[224:227], a[212:215], v[116:119]
ds_read_b128 a[192:195], v32 offset:0
v_mfma_f32_16x16x32_bf16 v[116:119], v[236:239], a[244:247], v[116:119]
buffer_load_dwordx4 a[56:59], v1, s[20:23], s91 offen offset:2048 sc1
s_waitcnt lgkmcnt(8)
v_mfma_f32_16x16x32_bf16 v[152:155], v[228:231], a[216:219], v[152:155]
ds_read_b128 a[224:227], v34 offset:0
v_mfma_f32_16x16x32_bf16 v[88:91], v[220:223], a[216:219], v[88:91]
s_waitcnt lgkmcnt(8)
v_mfma_f32_16x16x32_bf16 v[88:91], v[232:235], a[248:251], v[88:91]
ds_read_b128 a[196:199], v32 offset:2048
v_mfma_f32_16x16x32_bf16 v[120:123], v[224:227], a[216:219], v[120:123]
buffer_load_dwordx4 a[60:63], v1, s[20:23], s91 offen offset:3072 sc1
v_mfma_f32_16x16x32_bf16 v[120:123], v[236:239], a[248:251], v[120:123]
ds_read_b128 a[228:231], v34 offset:2048
s_waitcnt lgkmcnt(9)
v_mfma_f32_16x16x32_bf16 v[156:159], v[228:231], a[220:223], v[156:159]
v_mfma_f32_16x16x32_bf16 v[92:95], v[220:223], a[220:223], v[92:95]
s_add_u32 s91, s30, 16384
s_waitcnt lgkmcnt(8)
v_mfma_f32_16x16x32_bf16 v[92:95], v[232:235], a[252:255], v[92:95]
buffer_load_dwordx4 a[64:67], v1, s[20:23], s91 offen offset:0 sc1
v_mfma_f32_16x16x32_bf16 v[124:127], v[224:227], a[220:223], v[124:127]
v_mfma_f32_16x16x32_bf16 v[124:127], v[236:239], a[252:255], v[124:127]
s_waitcnt lgkmcnt(0)
s_barrier
v_mfma_f32_16x16x32_bf16 v[136:139], v[228:231], a[200:203], v[136:139]
ds_read_b128 a[208:211], v28 offset:40960
v_mfma_f32_16x16x32_bf16 v[72:75], v[220:223], a[200:203], v[72:75]
)A"
R"A(buffer_load_dwordx4 a[68:71], v1, s[20:23], s91 offen offset:1024 sc1
v_mfma_f32_16x16x32_bf16 v[72:75], v[232:235], a[232:235], v[72:75]
ds_read_b128 a[240:243], v30 offset:40960
v_mfma_f32_16x16x32_bf16 v[104:107], v[224:227], a[200:203], v[104:107]
buffer_load_dwordx4 a[72:75], v1, s[20:23], s91 offen offset:2048 sc1
v_mfma_f32_16x16x32_bf16 v[104:107], v[236:239], a[232:235], v[104:107]
ds_read_b128 a[212:215], v28 offset:43008
v_mfma_f32_16x16x32_bf16 v[140:143], v[228:231], a[204:207], v[140:143]
v_mfma_f32_16x16x32_bf16 v[76:79], v[220:223], a[204:207], v[76:79]
ds_read_b128 a[244:247], v30 offset:43008
v_mfma_f32_16x16x32_bf16 v[76:79], v[232:235], a[236:239], v[76:79]
buffer_load_dwordx4 a[76:79], v1, s[20:23], s91 offen offset:3072 sc1
v_mfma_f32_16x16x32_bf16 v[108:111], v[224:227], a[204:207], v[108:111]
ds_read_b128 a[216:219], v29 offset:40960
v_mfma_f32_16x16x32_bf16 v[108:111], v[236:239], a[236:239], v[108:111]
v_mfma_f32_16x16x32_bf16 v[128:131], v[228:231], a[192:195], v[128:131]
ds_read_b128 a[248:251], v31 offset:40960
s_add_u32 s91, s30, 20480
v_mfma_f32_16x16x32_bf16 v[64:67], v[220:223], a[192:195], v[64:67]
buffer_load_dwordx4 a[80:83], v1, s[20:23], s91 offen offset:0 sc1
v_mfma_f32_16x16x32_bf16 v[64:67], v[232:235], a[224:227], v[64:67]
ds_read_b128 a[220:223], v29 offset:43008
v_mfma_f32_16x16x32_bf16 v[96:99], v[224:227], a[192:195], v[96:99]
buffer_load_dwordx4 a[84:87], v1, s[20:23], s91 offen offset:1024 sc1
v_mfma_f32_16x16x32_bf16 v[96:99], v[236:239], a[224:227], v[96:99]
ds_read_b128 a[252:255], v31 offset:43008
v_mfma_f32_16x16x32_bf16 v[132:135], v[228:231], a[196:199], v[132:135]
v_mfma_f32_16x16x32_bf16 v[68:71], v[220:223], a[196:199], v[68:71]
v_mfma_f32_16x16x32_bf16 v[68:71], v[232:235], a[228:231], v[68:71]
buffer_load_dwordx4 a[88:91], v1, s[20:23], s91 offen offset:2048 sc1
v_mfma_f32_16x16x32_bf16 v[100:103], v[224:227], a[196:199], v[100:103]
v_mfma_f32_16x16x32_bf16 v[100:103], v[236:239], a[228:231], v[100:103]
s_waitcnt vmcnt(31) lgkmcnt(7)
v_mfma_f32_16x16x32_bf16 v[144:147], v[48:51], a[208:211], v[144:147]
ds_read_b128 a[200:203], v29 offset:32768
v_mfma_f32_16x16x32_bf16 v[80:83], v[40:43], a[208:211], v[80:83]
buffer_load_dwordx4 a[92:95], v1, s[20:23], s91 offen offset:3072 sc1
s_waitcnt vmcnt(31) lgkmcnt(7)
v_mfma_f32_16x16x32_bf16 v[80:83], v[52:55], a[240:243], v[80:83]
ds_read_b128 a[232:235], v31 offset:32768
v_mfma_f32_16x16x32_bf16 v[112:115], v[44:47], a[208:211], v[112:115]
s_waitcnt vmcnt(30)
v_mfma_f32_16x16x32_bf16 v[112:115], v[56:59], a[240:243], v[112:115]
ds_read_b128 a[204:207], v29 offset:34816
s_add_u32 s91, s30, 24576
s_waitcnt lgkmcnt(8)
v_mfma_f32_16x16x32_bf16 v[148:151], v[48:51], a[212:215], v[148:151]
buffer_load_dwordx4 a[96:99], v1, s[20:23], s91 offen offset:0 sc1
v_mfma_f32_16x16x32_bf16 v[84:87], v[40:43], a[212:215], v[84:87]
ds_read_b128 a[236:239], v31 offset:34816
s_waitcnt lgkmcnt(8)
v_mfma_f32_16x16x32_bf16 v[84:87], v[52:55], a[244:247], v[84:87]
v_mfma_f32_16x16x32_bf16 v[116:119], v[44:47], a[212:215], v[116:119]
ds_read_b128 a[192:195], v28 offset:32768
v_mfma_f32_16x16x32_bf16 v[116:119], v[56:59], a[244:247], v[116:119]
buffer_load_dwordx4 a[100:103], v1, s[20:23], s91 offen offset:1024 sc1
s_waitcnt lgkmcnt(8)
v_mfma_f32_16x16x32_bf16 v[152:155], v[48:51], a[216:219], v[152:155]
ds_read_b128 a[224:227], v30 offset:32768
v_mfma_f32_16x16x32_bf16 v[88:91], v[40:43], a[216:219], v[88:91]
s_waitcnt lgkmcnt(8)
v_mfma_f32_16x16x32_bf16 v[88:91], v[52:55], a[248:251], v[88:91]
ds_read_b128 a[196:199], v28 offset:34816
v_mfma_f32_16x16x32_bf16 v[120:123], v[44:47], a[216:219], v[120:123]
buffer_load_dwordx4 a[104:107], v1, s[20:23], s91 offen offset:2048 sc1
v_mfma_f32_16x16x32_bf16 v[120:123], v[56:59], a[248:251], v[120:123]
ds_read_b128 a[228:231], v30 offset:34816
s_waitcnt lgkmcnt(9)
v_mfma_f32_16x16x32_bf16 v[156:159], v[48:51], a[220:223], v[156:159]
v_mfma_f32_16x16x32_bf16 v[92:95], v[40:43], a[220:223], v[92:95]
s_waitcnt lgkmcnt(8)
v_mfma_f32_16x16x32_bf16 v[92:95], v[52:55], a[252:255], v[92:95]
buffer_load_dwordx4 a[108:111], v1, s[20:23], s91 offen offset:3072 sc1
v_mfma_f32_16x16x32_bf16 v[124:127], v[44:47], a[220:223], v[124:127]
v_mfma_f32_16x16x32_bf16 v[124:127], v[56:59], a[252:255], v[124:127]
s_waitcnt lgkmcnt(0)
s_barrier
v_mfma_f32_16x16x32_bf16 v[136:139], v[48:51], a[200:203], v[136:139]
ds_read_b128 a[208:211], v28 offset:8192
s_add_u32 s91, s30, 28672
v_mfma_f32_16x16x32_bf16 v[72:75], v[40:43], a[200:203], v[72:75]
buffer_load_dwordx4 a[112:115], v1, s[20:23], s91 offen offset:0 sc1
v_mfma_f32_16x16x32_bf16 v[72:75], v[52:55], a[232:235], v[72:75]
ds_read_b128 a[240:243], v30 offset:8192
v_mfma_f32_16x16x32_bf16 v[104:107], v[44:47], a[200:203], v[104:107]
buffer_load_dwordx4 a[116:119], v1, s[20:23], s91 offen offset:1024 sc1
v_mfma_f32_16x16x32_bf16 v[104:107], v[56:59], a[232:235], v[104:107]
ds_read_b128 a[212:215], v28 offset:10240
v_mfma_f32_16x16x32_bf16 v[140:143], v[48:51], a[204:207], v[140:143]
v_mfma_f32_16x16x32_bf16 v[76:79], v[40:43], a[204:207], v[76:79]
ds_read_b128 a[244:247], v30 offset:10240
v_mfma_f32_16x16x32_bf16 v[76:79], v[52:55], a[236:239], v[76:79]
buffer_load_dwordx4 a[120:123], v1, s[20:23], s91 offen offset:2048 sc1
v_mfma_f32_16x16x32_bf16 v[108:111], v[44:47], a[204:207], v[108:111]
ds_read_b128 a[216:219], v29 offset:8192
v_mfma_f32_16x16x32_bf16 v[108:111], v[56:59], a[236:239], v[108:111]
v_mfma_f32_16x16x32_bf16 v[128:131], v[48:51], a[192:195], v[128:131]
ds_read_b128 a[248:251], v31 offset:8192
v_mfma_f32_16x16x32_bf16 v[64:67], v[40:43], a[192:195], v[64:67]
buffer_load_dwordx4 a[124:127], v1, s[20:23], s91 offen offset:3072 sc1
v_mfma_f32_16x16x32_bf16 v[64:67], v[52:55], a[224:227], v[64:67]
ds_read_b128 a[220:223], v29 offset:10240
s_add_u32 s91, s30, 32768
v_mfma_f32_16x16x32_bf16 v[96:99], v[44:47], a[192:195], v[96:99]
buffer_load_dwordx4 a[128:131], v1, s[20:23], s91 offen offset:0 sc1
v_mfma_f32_16x16x32_bf16 v[96:99], v[56:59], a[224:227], v[96:99]
ds_read_b128 a[252:255], v31 offset:10240
v_mfma_f32_16x16x32_bf16 v[132:135], v[48:51], a[196:199], v[132:135]
v_mfma_f32_16x16x32_bf16 v[68:71], v[40:43], a[196:199], v[68:71]
v_mfma_f32_16x16x32_bf16 v[68:71], v[52:55], a[228:231], v[68:71]
buffer_load_dwordx4 a[132:135], v1, s[20:23], s91 offen offset:1024 sc1
v_mfma_f32_16x16x32_bf16 v[100:103], v[44:47], a[196:199], v[100:103]
v_mfma_f32_16x16x32_bf16 v[100:103], v[56:59], a[228:231], v[100:103]
s_waitcnt vmcnt(37) lgkmcnt(7)
v_mfma_f32_16x16x32_bf16 v[144:147], v[168:171], a[208:211], v[144:147]
ds_read_b128 a[200:203], v29 offset:0
v_mfma_f32_16x16x32_bf16 v[80:83], v[160:163], a[208:211], v[80:83]
ds_read_b128 a[232:235], v31 offset:0
s_waitcnt vmcnt(36) lgkmcnt(8)
v_mfma_f32_16x16x32_bf16 v[80:83], v[172:175], a[240:243], v[80:83]
ds_read_b128 a[204:207], v29 offset:2048
v_mfma_f32_16x16x32_bf16 v[112:115], v[164:167], a[208:211], v[112:115]
ds_read_b128 a[236:239], v31 offset:2048
s_waitcnt vmcnt(35)
v_mfma_f32_16x16x32_bf16 v[112:115], v[176:179], a[240:243], v[112:115]
ds_read_b128 a[192:195], v28 offset:0
s_waitcnt lgkmcnt(10)
v_mfma_f32_16x16x32_bf16 v[148:151], v[168:171], a[212:215], v[148:151]
ds_read_b128 a[224:227], v30 offset:0
v_mfma_f32_16x16x32_bf16 v[84:87], v[160:163], a[212:215], v[84:87]
ds_read_b128 a[196:199], v28 offset:2048
s_waitcnt lgkmcnt(11)
v_mfma_f32_16x16x32_bf16 v[84:87], v[172:175], a[244:247], v[84:87]
ds_read_b128 a[228:231], v30 offset:2048
v_mfma_f32_16x16x32_bf16 v[116:119], v[164:167], a[212:215], v[116:119]
ds_write_b128 v20, v[144:147] offset:10240
v_mfma_f32_16x16x32_bf16 v[116:119], v[176:179], a[244:247], v[116:119]
buffer_load_dwordx4 a[136:139], v1, s[20:23], s91 offen offset:2048 sc1
s_waitcnt lgkmcnt(12)
v_mfma_f32_16x16x32_bf16 v[152:155], v[168:171], a[216:219], v[152:155]
ds_write_b128 v20, v[80:83] offset:0
v_mfma_f32_16x16x32_bf16 v[88:91], v[160:163], a[216:219], v[88:91]
buffer_load_dwordx4 a[140:143], v1, s[20:23], s91 offen offset:3072 sc1
)A"
R"A(s_waitcnt lgkmcnt(12)
v_mfma_f32_16x16x32_bf16 v[88:91], v[172:175], a[248:251], v[88:91]
ds_write_b128 v20, v[112:115] offset:4096
v_mfma_f32_16x16x32_bf16 v[120:123], v[164:167], a[216:219], v[120:123]
v_mfma_f32_16x16x32_bf16 v[120:123], v[176:179], a[248:251], v[120:123]
ds_write_b128 v20, v[148:151] offset:11264
s_add_u32 s91, s30, 36864
s_waitcnt lgkmcnt(13)
v_mfma_f32_16x16x32_bf16 v[156:159], v[168:171], a[220:223], v[156:159]
buffer_load_dwordx4 a[144:147], v1, s[20:23], s91 offen offset:0 sc1
v_mfma_f32_16x16x32_bf16 v[92:95], v[160:163], a[220:223], v[92:95]
ds_write_b128 v20, v[84:87] offset:1024
s_waitcnt lgkmcnt(13)
v_mfma_f32_16x16x32_bf16 v[92:95], v[172:175], a[252:255], v[92:95]
buffer_load_dwordx4 a[148:151], v1, s[20:23], s91 offen offset:1024 sc1
v_mfma_f32_16x16x32_bf16 v[124:127], v[164:167], a[220:223], v[124:127]
ds_write_b128 v20, v[116:119] offset:5120
v_mfma_f32_16x16x32_bf16 v[124:127], v[176:179], a[252:255], v[124:127]
s_waitcnt lgkmcnt(13)
v_mfma_f32_16x16x32_bf16 v[136:139], v[168:171], a[200:203], v[136:139]
ds_write_b128 v20, v[152:155] offset:12288
v_mfma_f32_16x16x32_bf16 v[72:75], v[160:163], a[200:203], v[72:75]
buffer_load_dwordx4 a[152:155], v1, s[20:23], s91 offen offset:2048 sc1
s_waitcnt lgkmcnt(13)
v_mfma_f32_16x16x32_bf16 v[72:75], v[172:175], a[232:235], v[72:75]
ds_write_b128 v20, v[88:91] offset:2048
v_mfma_f32_16x16x32_bf16 v[104:107], v[164:167], a[200:203], v[104:107]
v_mfma_f32_16x16x32_bf16 v[104:107], v[176:179], a[232:235], v[104:107]
ds_write_b128 v20, v[120:123] offset:6144
s_waitcnt lgkmcnt(14)
v_mfma_f32_16x16x32_bf16 v[140:143], v[168:171], a[204:207], v[140:143]
buffer_load_dwordx4 a[156:159], v1, s[20:23], s91 offen offset:3072 sc1
v_mfma_f32_16x16x32_bf16 v[76:79], v[160:163], a[204:207], v[76:79]
ds_write_b128 v20, v[156:159] offset:13312
s_add_u32 s91, s30, 40960
s_waitcnt lgkmcnt(14)
v_mfma_f32_16x16x32_bf16 v[76:79], v[172:175], a[236:239], v[76:79]
buffer_load_dwordx4 a[160:163], v1, s[20:23], s91 offen offset:0 sc1
v_mfma_f32_16x16x32_bf16 v[108:111], v[164:167], a[204:207], v[108:111]
ds_write_b128 v20, v[92:95] offset:3072
v_mfma_f32_16x16x32_bf16 v[108:111], v[176:179], a[236:239], v[108:111]
s_waitcnt lgkmcnt(14)
v_mfma_f32_16x16x32_bf16 v[128:131], v[168:171], a[192:195], v[128:131]
ds_write_b128 v20, v[124:127] offset:7168
v_mfma_f32_16x16x32_bf16 v[64:67], v[160:163], a[192:195], v[64:67]
buffer_load_dwordx4 a[164:167], v1, s[20:23], s91 offen offset:1024 sc1
s_waitcnt lgkmcnt(14)
v_mfma_f32_16x16x32_bf16 v[64:67], v[172:175], a[224:227], v[64:67]
ds_write_b128 v20, v[136:139] offset:8192
v_mfma_f32_16x16x32_bf16 v[96:99], v[164:167], a[192:195], v[96:99]
v_mfma_f32_16x16x32_bf16 v[96:99], v[176:179], a[224:227], v[96:99]
ds_write_b128 v20, v[140:143] offset:9216
s_waitcnt lgkmcnt(15)
v_mfma_f32_16x16x32_bf16 v[132:135], v[168:171], a[196:199], v[132:135]
buffer_load_dwordx4 a[168:171], v1, s[20:23], s91 offen offset:2048 sc1
v_mfma_f32_16x16x32_bf16 v[68:71], v[160:163], a[196:199], v[68:71]
buffer_load_dwordx4 a[172:175], v1, s[20:23], s91 offen offset:3072 sc1
s_waitcnt lgkmcnt(14)
v_mfma_f32_16x16x32_bf16 v[68:71], v[172:175], a[228:231], v[68:71]
s_add_u32 s91, s30, 45056
v_mfma_f32_16x16x32_bf16 v[100:103], v[164:167], a[196:199], v[100:103]
buffer_load_dwordx4 a[176:179], v1, s[20:23], s91 offen offset:0 sc1
v_mfma_f32_16x16x32_bf16 v[100:103], v[176:179], a[228:231], v[100:103]
s_waitcnt lgkmcnt(0)
s_barrier
ds_read_b128 v[160:163], v21 offset:0
ds_read_b128 v[164:167], v21 offset:4096
ds_read_b128 v[168:171], v21 offset:1024
ds_read_b128 v[172:175], v21 offset:5120
ds_read_b128 v[176:179], v21 offset:2048
ds_read_b128 v[180:183], v21 offset:6144
ds_read_b128 v[184:187], v21 offset:3072
ds_read_b128 v[188:191], v21 offset:7168
ds_read_b128 v[192:195], v22 offset:8192
ds_read_b128 v[196:199], v22 offset:9216
ds_read_b128 v[200:203], v21 offset:10240
ds_read_b128 v[204:207], v21 offset:11264
ds_read_b128 v[208:211], v23 offset:12288
ds_read_b128 v[212:215], v23 offset:13312
s_waitcnt lgkmcnt(13)
v_pk_add_f32 v[64:65], v[64:65], v[160:161]
v_pk_add_f32 v[66:67], v[66:67], v[162:163]
s_waitcnt lgkmcnt(12)
v_pk_add_f32 v[96:97], v[96:97], v[164:165]
v_pk_add_f32 v[98:99], v[98:99], v[166:167]
v_cvt_pk_bf16_f32 v160, v64, v65
v_cvt_pk_bf16_f32 v161, v66, v67
v_cvt_pk_bf16_f32 v162, v96, v97
v_cvt_pk_bf16_f32 v163, v98, v99
buffer_store_dwordx4 v[160:163], v1, s[20:23], s56 offen offset:0 sc1
s_waitcnt lgkmcnt(11)
v_pk_add_f32 v[68:69], v[68:69], v[168:169]
v_pk_add_f32 v[70:71], v[70:71], v[170:171]
s_waitcnt lgkmcnt(10)
v_pk_add_f32 v[100:101], v[100:101], v[172:173]
v_pk_add_f32 v[102:103], v[102:103], v[174:175]
v_cvt_pk_bf16_f32 v168, v68, v69
v_cvt_pk_bf16_f32 v169, v70, v71
v_cvt_pk_bf16_f32 v170, v100, v101
v_cvt_pk_bf16_f32 v171, v102, v103
buffer_store_dwordx4 v[168:171], v1, s[20:23], s57 offen offset:0 sc1
s_waitcnt lgkmcnt(9)
v_pk_add_f32 v[72:73], v[72:73], v[176:177]
v_pk_add_f32 v[74:75], v[74:75], v[178:179]
s_waitcnt lgkmcnt(8)
v_pk_add_f32 v[104:105], v[104:105], v[180:181]
v_pk_add_f32 v[106:107], v[106:107], v[182:183]
v_cvt_pk_bf16_f32 v176, v72, v73
v_cvt_pk_bf16_f32 v177, v74, v75
v_cvt_pk_bf16_f32 v178, v104, v105
v_cvt_pk_bf16_f32 v179, v106, v107
buffer_store_dwordx4 v[176:179], v1, s[20:23], s58 offen offset:0 sc1
s_waitcnt lgkmcnt(7)
v_pk_add_f32 v[76:77], v[76:77], v[184:185]
v_pk_add_f32 v[78:79], v[78:79], v[186:187]
s_waitcnt lgkmcnt(6)
v_pk_add_f32 v[108:109], v[108:109], v[188:189]
v_pk_add_f32 v[110:111], v[110:111], v[190:191]
v_cvt_pk_bf16_f32 v184, v76, v77
v_cvt_pk_bf16_f32 v185, v78, v79
v_cvt_pk_bf16_f32 v186, v108, v109
v_cvt_pk_bf16_f32 v187, v110, v111
buffer_store_dwordx4 v[184:187], v1, s[20:23], s59 offen offset:0 sc1
s_waitcnt lgkmcnt(5)
v_pk_add_f32 v[128:129], v[128:129], v[192:193]
v_pk_add_f32 v[130:131], v[130:131], v[194:195]
s_waitcnt lgkmcnt(4)
v_pk_add_f32 v[132:133], v[132:133], v[196:197]
v_pk_add_f32 v[134:135], v[134:135], v[198:199]
s_waitcnt lgkmcnt(3)
v_pk_add_f32 v[128:129], v[128:129], v[200:201]
v_pk_add_f32 v[130:131], v[130:131], v[202:203]
s_waitcnt lgkmcnt(2)
v_pk_add_f32 v[132:133], v[132:133], v[204:205]
v_pk_add_f32 v[134:135], v[134:135], v[206:207]
s_waitcnt lgkmcnt(1)
v_pk_add_f32 v[128:129], v[128:129], v[208:209]
v_pk_add_f32 v[130:131], v[130:131], v[210:211]
s_waitcnt lgkmcnt(0)
v_pk_add_f32 v[132:133], v[132:133], v[212:213]
v_pk_add_f32 v[134:135], v[134:135], v[214:215]
v_cvt_pk_bf16_f32 v196, v128, v129
v_cvt_pk_bf16_f32 v197, v130, v131
v_cvt_pk_bf16_f32 v198, v132, v133
v_cvt_pk_bf16_f32 v199, v134, v135
buffer_store_dwordx4 v[196:199], v1, s[20:23], s13 offen offset:0 sc1
s_waitcnt vmcnt(49)
v_mfma_f32_16x16x32_bf16 a[240:243], a[0:3], v[60:63], 0
buffer_load_dwordx4 a[180:183], v1, s[20:23], s91 offen offset:1024 sc1
s_waitcnt vmcnt(47)
v_mfma_f32_16x16x32_bf16 a[240:243], a[12:15], v[60:63], a[240:243]
s_mov_b32 s44, s62
s_and_b32 s45, s63, 0xffff
s_waitcnt vmcnt(44)
v_mfma_f32_16x16x32_bf16 a[240:243], a[24:27], v[60:63], a[240:243]
buffer_load_dwordx4 a[184:187], v1, s[20:23], s91 offen offset:2048 sc1
s_waitcnt vmcnt(42)
v_mfma_f32_16x16x32_bf16 a[240:243], a[36:39], v[60:63], a[240:243]
s_mov_b32 s46, 1966080
s_mov_b32 s47, 0x20000
s_waitcnt vmcnt(39)
v_mfma_f32_16x16x32_bf16 a[240:243], a[48:51], v[60:63], a[240:243]
s_waitcnt vmcnt(36)
v_mfma_f32_16x16x32_bf16 a[240:243], a[60:63], v[60:63], a[240:243]
s_lshl_b32 s56, s16, 15
s_add_u32 s56, s56, 32768
s_waitcnt vmcnt(33)
v_mfma_f32_16x16x32_bf16 a[240:243], a[72:75], v[60:63], a[240:243]
buffer_load_dwordx4 a[188:191], v1, s[20:23], s91 offen offset:3072 sc1
s_waitcnt vmcnt(31)
v_mfma_f32_16x16x32_bf16 a[240:243], a[84:87], v[60:63], a[240:243]
s_mul_i32 s57, s16, 491520
v_lshlrev_b32_e32 v6, 1, v4
s_waitcnt vmcnt(28)
v_mfma_f32_16x16x32_bf16 a[240:243], a[96:99], v[60:63], a[240:243]
s_waitcnt vmcnt(25)
v_mfma_f32_16x16x32_bf16 a[240:243], a[108:111], v[60:63], a[240:243]
v_and_b32_e32 v6, 4, v6
v_and_b32_e32 v7, 7, v0
s_add_u32 s91, s30, 49152
)A"
R"A(s_waitcnt vmcnt(22)
v_mfma_f32_16x16x32_bf16 a[240:243], a[120:123], v[60:63], a[240:243]
buffer_load_dwordx4 a[192:195], v1, s[20:23], s91 offen offset:0 sc1
s_waitcnt vmcnt(20)
v_mfma_f32_16x16x32_bf16 a[240:243], a[132:135], v[60:63], a[240:243]
v_xor_b32_e32 v6, v6, v7
v_lshlrev_b32_e32 v6, 4, v6
s_waitcnt vmcnt(17)
v_mfma_f32_16x16x32_bf16 a[240:243], a[144:147], v[60:63], a[240:243]
s_waitcnt vmcnt(14)
v_mfma_f32_16x16x32_bf16 a[240:243], a[156:159], v[60:63], a[240:243]
s_movk_i32 s89, 1280
v_mad_u32_u24 v6, v4, s89, v6
s_waitcnt vmcnt(11)
v_mfma_f32_16x16x32_bf16 a[240:243], a[168:171], v[60:63], a[240:243]
buffer_load_dwordx4 a[196:199], v1, s[20:23], s91 offen offset:1024 sc1
v_mfma_f32_16x16x32_bf16 a[244:247], a[4:7], v[60:63], 0
s_lshl_b32 s88, s24, 7
v_add_u32_e32 v4, s88, v6
v_mfma_f32_16x16x32_bf16 a[244:247], a[16:19], v[60:63], a[244:247]
v_mfma_f32_16x16x32_bf16 a[244:247], a[28:31], v[60:63], a[244:247]
v_lshrrev_b32_e32 v6, 2, v0
v_and_b32_e32 v7, 3, v0
v_mfma_f32_16x16x32_bf16 a[244:247], a[40:43], v[60:63], a[244:247]
buffer_load_dwordx4 a[200:203], v1, s[20:23], s91 offen offset:2048 sc1
v_mfma_f32_16x16x32_bf16 a[244:247], a[52:55], v[60:63], a[244:247]
v_and_b32_e32 v12, 8, v0
v_lshlrev_b32_e32 v13, 7, v6
v_mfma_f32_16x16x32_bf16 a[244:247], a[64:67], v[60:63], a[244:247]
v_mfma_f32_16x16x32_bf16 a[244:247], a[76:79], v[60:63], a[244:247]
v_lshl_add_u32 v13, v12, 3, v13
v_mfma_f32_16x16x32_bf16 a[244:247], a[88:91], v[60:63], a[244:247]
buffer_load_dwordx4 a[204:207], v1, s[20:23], s91 offen offset:3072 sc1
v_mfma_f32_16x16x32_bf16 a[244:247], a[100:103], v[60:63], a[244:247]
v_lshl_add_u32 v13, v7, 4, v13
s_lshl_b32 s88, s75, 6
v_mfma_f32_16x16x32_bf16 a[244:247], a[112:115], v[60:63], a[244:247]
v_mfma_f32_16x16x32_bf16 a[244:247], a[124:127], v[60:63], a[244:247]
v_xor_b32_e32 v13, s88, v13
v_add_u32_e32 v5, s56, v13
s_add_u32 s91, s30, 53248
v_mfma_f32_16x16x32_bf16 a[244:247], a[136:139], v[60:63], a[244:247]
buffer_load_dwordx4 a[208:211], v1, s[20:23], s91 offen offset:0 sc1
v_mfma_f32_16x16x32_bf16 a[244:247], a[148:151], v[60:63], a[244:247]
s_lshl_b32 s88, s75, 11
v_add_u32_e32 v14, s88, v1
v_mfma_f32_16x16x32_bf16 a[244:247], a[160:163], v[60:63], a[244:247]
s_waitcnt vmcnt(14)
v_mfma_f32_16x16x32_bf16 a[244:247], a[172:175], v[60:63], a[244:247]
v_xor_b32_e32 v15, 2048, v14
v_add_u32_e32 v252, 4096, v1
v_mfma_f32_16x16x32_bf16 a[248:251], a[8:11], v[60:63], 0
buffer_load_dwordx4 a[212:215], v1, s[20:23], s91 offen offset:1024 sc1
v_mfma_f32_16x16x32_bf16 a[248:251], a[20:23], v[60:63], a[248:251]
v_lshrrev_b32_e32 v6, 2, v2
v_cmp_eq_u32_e64 s[40:41], v6, v3
v_mfma_f32_16x16x32_bf16 a[248:251], a[32:35], v[60:63], a[248:251]
v_mfma_f32_16x16x32_bf16 a[248:251], a[44:47], v[60:63], a[248:251]
v_and_b32_e32 v6, 1, v0
v_cmp_eq_u32_e64 s[42:43], 1, v6
v_mfma_f32_16x16x32_bf16 a[248:251], a[56:59], v[60:63], a[248:251]
buffer_load_dwordx4 a[216:219], v1, s[20:23], s91 offen offset:2048 sc1
v_mfma_f32_16x16x32_bf16 a[248:251], a[68:71], v[60:63], a[248:251]
v_and_b32_e32 v6, 2, v0
v_cmp_eq_u32_e64 s[52:53], 2, v6
v_mfma_f32_16x16x32_bf16 a[248:251], a[80:83], v[60:63], a[248:251]
v_mfma_f32_16x16x32_bf16 a[248:251], a[92:95], v[60:63], a[248:251]
v_lshlrev_b32_e32 v253, 5, v2
s_mov_b32 s54, 0x3acccccd
v_mfma_f32_16x16x32_bf16 a[248:251], a[104:107], v[60:63], a[248:251]
buffer_load_dwordx4 a[220:223], v1, s[20:23], s91 offen offset:3072 sc1
v_mfma_f32_16x16x32_bf16 a[248:251], a[116:119], v[60:63], a[248:251]
s_mov_b32 s55, 0x3727c5ac
s_lshr_b32 s58, s24, 2
v_mfma_f32_16x16x32_bf16 a[248:251], a[128:131], v[60:63], a[248:251]
v_mfma_f32_16x16x32_bf16 a[248:251], a[140:143], v[60:63], a[248:251]
s_lshl_b32 s88, s58, 10
v_add_u32_e32 v12, s88, v1
s_add_u32 s91, s30, 57344
v_mfma_f32_16x16x32_bf16 a[248:251], a[152:155], v[60:63], a[248:251]
buffer_load_dwordx4 a[224:227], v1, s[20:23], s91 offen offset:0 sc1
v_mfma_f32_16x16x32_bf16 a[248:251], a[164:167], v[60:63], a[248:251]
v_xor_b32_e32 v13, 1024, v12
s_waitcnt vmcnt(17)
v_mfma_f32_16x16x32_bf16 a[248:251], a[176:179], v[60:63], a[248:251]
s_waitcnt vmcnt(12)
s_barrier
s_cmp_lg_u32 s16, 0
s_cbranch_scc1 Lpb6_%=
v_mov_b32_e32 v244, s70
v_mov_b32_e32 v245, s71
s_mov_b64 exec, 1
buffer_store_dwordx2 v[244:245], off, s[20:23], s78 sc1
s_mov_b64 exec, -1
Lpb6_%=:
buffer_load_dwordx4 a[228:231], v1, s[20:23], s91 offen offset:1024 sc1
buffer_load_dwordx4 a[232:235], v1, s[20:23], s91 offen offset:2048 sc1
buffer_load_dwordx4 a[236:239], v1, s[20:23], s91 offen offset:3072 sc1
global_load_dwordx2 v[34:35], v251, s[60:61] offset:0
global_load_dwordx2 v[38:39], v251, s[60:61] offset:32
global_load_dwordx2 v[42:43], v251, s[60:61] offset:64
s_add_u32 m0, s56, 0
s_add_u32 s93, s57, 184320
buffer_load_dwordx4 v4, s[44:47], s93 offen offset:0 lds
s_add_u32 s93, s93, 9216
buffer_load_dwordx4 v4, s[44:47], s93 offen offset:1024 lds
s_add_u32 s93, s93, 9216
buffer_load_dwordx4 v4, s[44:47], s93 offen offset:2048 lds
s_add_u32 s93, s93, 9216
buffer_load_dwordx4 v4, s[44:47], s93 offen offset:3072 lds
s_add_u32 m0, s56, 4096
s_add_u32 s93, s57, 163840
buffer_load_dwordx4 v4, s[44:47], s93 offen offset:0 lds
s_add_u32 s93, s93, 9216
buffer_load_dwordx4 v4, s[44:47], s93 offen offset:1024 lds
s_add_u32 s93, s93, 50176
buffer_load_dwordx4 v4, s[44:47], s93 offen offset:2048 lds
s_add_u32 s93, s93, 9216
buffer_load_dwordx4 v4, s[44:47], s93 offen offset:3072 lds
v_mov_b32_e32 v6, 0x3f80
v_mov_b32_e32 v7, 0x3f800000
v_cndmask_b32_e64 v6, v6, v7, s[42:43]
v_cndmask_b32_e64 v6, 0, v6, s[40:41]
v_cndmask_b32_e64 v8, v6, 0, s[52:53]
v_cndmask_b32_e64 v9, 0, v6, s[52:53]
v_lshlrev_b32_e32 v6, 2, v8
v_and_b32_e32 v6, 0x80008000, v6
v_or_b32_e32 v10, v6, v8
v_lshlrev_b32_e32 v6, 2, v9
v_and_b32_e32 v6, 0x80008000, v6
v_or_b32_e32 v11, v6, v9
s_lshl_b32 s88, s16, 3
v_add_u32_e32 v251, s88, v253
s_waitcnt vmcnt(25)
v_mfma_f32_16x16x32_bf16 a[240:243], a[180:183], v[60:63], a[240:243]
s_waitcnt vmcnt(22)
v_mfma_f32_16x16x32_bf16 a[240:243], a[192:195], v[60:63], a[240:243]
s_waitcnt vmcnt(19)
v_mfma_f32_16x16x32_bf16 a[240:243], a[204:207], v[60:63], a[240:243]
s_waitcnt vmcnt(16)
v_mfma_f32_16x16x32_bf16 a[240:243], a[216:219], v[60:63], a[240:243]
s_waitcnt vmcnt(13)
v_mfma_f32_16x16x32_bf16 a[240:243], a[228:231], v[60:63], a[240:243]
v_mfma_f32_16x16x32_bf16 a[244:247], a[184:187], v[60:63], a[244:247]
v_mfma_f32_16x16x32_bf16 a[244:247], a[196:199], v[60:63], a[244:247]
v_mfma_f32_16x16x32_bf16 a[244:247], a[208:211], v[60:63], a[244:247]
v_mfma_f32_16x16x32_bf16 a[244:247], a[220:223], v[60:63], a[244:247]
s_waitcnt vmcnt(12)
v_mfma_f32_16x16x32_bf16 a[244:247], a[232:235], v[60:63], a[244:247]
v_mfma_f32_16x16x32_bf16 a[248:251], a[188:191], v[60:63], a[248:251]
v_mfma_f32_16x16x32_bf16 a[248:251], a[200:203], v[60:63], a[248:251]
v_mfma_f32_16x16x32_bf16 a[248:251], a[212:215], v[60:63], a[248:251]
v_mfma_f32_16x16x32_bf16 a[248:251], a[224:227], v[60:63], a[248:251]
s_waitcnt vmcnt(11)
v_mfma_f32_16x16x32_bf16 a[248:251], a[236:239], v[60:63], a[248:251]
buffer_load_dwordx2 v[244:245], v246, s[20:23], s79 offen sc1
s_add_u32 m0, s56, 8192
s_add_u32 s93, s57, 245760
buffer_load_dwordx4 v4, s[44:47], s93 offen offset:0 lds
s_add_u32 s93, s93, 9216
buffer_load_dwordx4 v4, s[44:47], s93 offen offset:1024 lds
s_add_u32 s93, s93, 9216
buffer_load_dwordx4 v4, s[44:47], s93 offen offset:2048 lds
s_add_u32 s93, s93, 9216
buffer_load_dwordx4 v4, s[44:47], s93 offen offset:3072 lds
s_add_u32 m0, s56, 12288
s_add_u32 s93, s57, 307200
buffer_load_dwordx4 v4, s[44:47], s93 offen offset:0 lds
s_add_u32 s93, s93, 9216
buffer_load_dwordx4 v4, s[44:47], s93 offen offset:1024 lds
s_add_u32 s93, s93, 9216
buffer_load_dwordx4 v4, s[44:47], s93 offen offset:2048 lds
s_add_u32 s93, s93, 9216
buffer_load_dwordx4 v4, s[44:47], s93 offen offset:3072 lds
s_add_u32 s90, s83, 0
s_add_u32 s92, s84, 0
s_cmp_lg_u32 s16, 0
s_cbranch_scc1 Lpd7_%=
s_mov_b32 s8, 0x200000
s_waitcnt vmcnt(8)
v_cmp_ne_u64_e32 vcc, s[70:71], v[244:245]
)A"
R"A(s_cmp_eq_u64 vcc, 0
s_cbranch_scc1 Lpd7_%=
Lpl7_%=:
buffer_load_dwordx2 v[244:245], v246, s[20:23], s79 offen sc1
s_waitcnt vmcnt(0)
v_cmp_ne_u64_e32 vcc, s[70:71], v[244:245]
s_cmp_eq_u64 vcc, 0
s_cbranch_scc1 Lpd7_%=
s_sub_u32 s8, s8, 1
s_cbranch_scc0 Lpl7_%=
Lpd7_%=:
s_barrier
buffer_load_dwordx4 v[176:179], v247, s[20:23], s90 offen offset:0 sc1
buffer_load_dwordx4 v[184:187], v247, s[20:23], s92 offen offset:0 sc1
buffer_load_dwordx4 v[180:183], v248, s[20:23], s90 offen offset:0 sc1
buffer_load_dwordx4 v[188:191], v248, s[20:23], s92 offen offset:0 sc1
s_add_u32 s90, s83, 4096
s_add_u32 s92, s84, 4096
buffer_load_dwordx4 v[192:195], v247, s[20:23], s90 offen offset:0 sc1
buffer_load_dwordx4 v[200:203], v247, s[20:23], s92 offen offset:0 sc1
buffer_load_dwordx4 v[196:199], v248, s[20:23], s90 offen offset:0 sc1
buffer_load_dwordx4 v[204:207], v248, s[20:23], s92 offen offset:0 sc1
s_add_u32 s90, s83, 8192
s_add_u32 s92, s84, 8192
buffer_load_dwordx4 v[208:211], v247, s[20:23], s90 offen offset:0 sc1
buffer_load_dwordx4 v[216:219], v247, s[20:23], s92 offen offset:0 sc1
buffer_load_dwordx4 v[212:215], v248, s[20:23], s90 offen offset:0 sc1
buffer_load_dwordx4 v[220:223], v248, s[20:23], s92 offen offset:0 sc1
s_waitcnt vmcnt(31)
v_lshlrev_b32_e32 v32, 16, v34
v_and_b32_e32 v33, s98, v34
v_lshlrev_b32_e32 v34, 16, v35
v_and_b32_e32 v35, s98, v35
s_waitcnt vmcnt(30)
v_lshlrev_b32_e32 v36, 16, v38
v_and_b32_e32 v37, s98, v38
v_lshlrev_b32_e32 v38, 16, v39
v_and_b32_e32 v39, s98, v39
s_waitcnt vmcnt(29)
v_lshlrev_b32_e32 v40, 16, v42
v_and_b32_e32 v41, s98, v42
v_lshlrev_b32_e32 v42, 16, v43
v_and_b32_e32 v43, s98, v43
v_accvgpr_read_b32 v240, a240
v_accvgpr_read_b32 v241, a241
v_accvgpr_read_b32 v242, a242
v_accvgpr_read_b32 v243, a243
v_accvgpr_read_b32 v56, a244
v_accvgpr_read_b32 v57, a245
v_accvgpr_read_b32 v58, a246
v_accvgpr_read_b32 v59, a247
v_accvgpr_read_b32 v44, a248
v_accvgpr_read_b32 v45, a249
v_accvgpr_read_b32 v46, a250
v_accvgpr_read_b32 v47, a251
s_lshl_b32 s88, s24, 7
s_lshl_b32 s89, s75, 6
s_add_u32 s88, s88, s89
v_lshl_add_u32 v246, v3, 4, s88
s_lshl_b32 s88, s24, 5
s_add_u32 s88, s88, 1024
v_lshl_add_u32 v3, v3, 3, s88
s_lshr_b32 s59, s24, 1
s_lshl_b32 s88, s59, 1
s_add_u32 s88, s88, s74
s_lshl_b32 s88, s88, 10
s_and_b32 s89, s24, 1
s_lshl_b32 s12, s89, 3
s_add_u32 s12, s12, s88
s_lshl_b32 s89, s89, 1
s_add_u32 s89, s89, s75
s_lshl_b32 s89, s89, 1
s_add_u32 s89, s89, s74
s_lshl_b32 s89, s89, 10
s_waitcnt vmcnt(20)
v_add_u32_e32 v245, s89, v1
s_waitcnt vmcnt(8)
v_swap_b32 v178, v184
v_swap_b32 v179, v185
v_swap_b32 v182, v188
v_swap_b32 v183, v189
v_mfma_f32_16x16x32_bf16 v[16:19], v[8:11], v[176:179], 0
v_mfma_f32_16x16x32_bf16 v[20:23], v[8:11], v[184:187], 0
v_mfma_f32_16x16x32_bf16 v[24:27], v[8:11], v[180:183], 0
v_mfma_f32_16x16x32_bf16 v[28:31], v[8:11], v[188:191], 0
s_waitcnt vmcnt(6)
v_swap_b32 v194, v200
v_swap_b32 v195, v201
s_waitcnt vmcnt(4)
v_swap_b32 v198, v204
v_swap_b32 v199, v205
v_cvt_pk_bf16_f32 v48, v16, v17
v_cvt_pk_bf16_f32 v49, v18, v19
v_cvt_pk_bf16_f32 v50, v20, v21
v_cvt_pk_bf16_f32 v51, v22, v23
ds_write_b128 v249, v[48:51] offset:0
v_cvt_pk_bf16_f32 v52, v24, v25
v_cvt_pk_bf16_f32 v53, v26, v27
v_cvt_pk_bf16_f32 v54, v28, v29
v_cvt_pk_bf16_f32 v55, v30, v31
ds_write_b128 v249, v[52:55] offset:1024
s_add_u32 s90, s83, 12288
s_add_u32 s92, s84, 12288
v_mfma_f32_16x16x32_bf16 v[112:115], v[60:63], v[48:51], 0
buffer_load_dwordx4 v[176:179], v247, s[20:23], s90 offen offset:0 sc1
v_mfma_f32_16x16x32_bf16 v[120:123], v[48:51], v[48:51], 0
buffer_load_dwordx4 v[184:187], v247, s[20:23], s92 offen offset:0 sc1
v_mfma_f32_16x16x32_bf16 v[116:119], v[60:63], v[52:55], 0
buffer_load_dwordx4 v[180:183], v248, s[20:23], s90 offen offset:0 sc1
v_mfma_f32_16x16x32_bf16 v[124:127], v[52:55], v[52:55], 0
buffer_load_dwordx4 v[188:191], v248, s[20:23], s92 offen offset:0 sc1
s_waitcnt lgkmcnt(0)
s_barrier
s_cmp_eq_u32 s59, 0
s_cselect_b64 exec, -1, 0
ds_read_b128 a[240:243], v245 offset:0
s_mov_b64 exec, -1
ds_read_b128 v[128:131], v1 offset:0
ds_read_b128 v[132:135], v1 offset:1024
ds_read_b128 v[136:139], v1 offset:2048
ds_read_b128 v[140:143], v1 offset:3072
ds_read_b128 v[144:147], v1 offset:4096
ds_read_b128 v[148:151], v1 offset:5120
ds_read_b128 v[152:155], v1 offset:6144
ds_read_b128 v[156:159], v1 offset:7168
s_add_u32 m0, s56, 16384
s_add_u32 s93, s57, 286720
v_mfma_f32_16x16x32_bf16 v[16:19], v[8:11], v[192:195], 0
buffer_load_dwordx4 v4, s[44:47], s93 offen offset:0 lds
v_mfma_f32_16x16x32_bf16 v[20:23], v[8:11], v[200:203], 0
s_add_u32 s93, s93, 9216
v_mfma_f32_16x16x32_bf16 v[24:27], v[8:11], v[196:199], 0
buffer_load_dwordx4 v4, s[44:47], s93 offen offset:1024 lds
v_mfma_f32_16x16x32_bf16 v[28:31], v[8:11], v[204:207], 0
s_add_u32 s90, s83, 32768
s_add_u32 s92, s84, 32768
s_waitcnt lgkmcnt(7)
v_mfma_f32_16x16x32_bf16 v[64:67], a[0:3], v[128:131], 0
buffer_load_dwordx4 v[192:195], v249, s[20:23], s90 offen offset:0 sc1
s_waitcnt lgkmcnt(5)
v_mfma_f32_16x16x32_bf16 v[64:67], a[12:15], v[136:139], v[64:67]
v_cvt_pk_bf16_f32 v48, v16, v17
v_cvt_pk_bf16_f32 v49, v18, v19
v_mfma_f32_16x16x32_bf16 v[72:75], a[4:7], v[128:131], 0
v_cvt_pk_bf16_f32 v50, v20, v21
v_cvt_pk_bf16_f32 v51, v22, v23
v_mfma_f32_16x16x32_bf16 v[72:75], a[16:19], v[136:139], v[72:75]
ds_write_b128 v249, v[48:51] offset:8192
v_mfma_f32_16x16x32_bf16 v[80:83], a[8:11], v[128:131], 0
v_cvt_pk_bf16_f32 v52, v24, v25
v_cvt_pk_bf16_f32 v53, v26, v27
v_mfma_f32_16x16x32_bf16 v[80:83], a[20:23], v[136:139], v[80:83]
v_cvt_pk_bf16_f32 v54, v28, v29
v_cvt_pk_bf16_f32 v55, v30, v31
v_mfma_f32_16x16x32_bf16 v[112:115], v[60:63], v[48:51], v[112:115]
ds_write_b128 v249, v[52:55] offset:9216
v_mfma_f32_16x16x32_bf16 v[120:123], v[48:51], v[48:51], v[120:123]
buffer_load_dwordx4 v[200:203], v249, s[20:23], s92 offen offset:0 sc1
v_mfma_f32_16x16x32_bf16 v[68:71], a[0:3], v[132:135], 0
v_mfma_f32_16x16x32_bf16 v[116:119], v[60:63], v[52:55], v[116:119]
buffer_load_dwordx4 v[196:199], v249, s[20:23], s90 offen offset:1024 sc1
v_mfma_f32_16x16x32_bf16 v[124:127], v[52:55], v[52:55], v[124:127]
s_waitcnt lgkmcnt(6)
v_mfma_f32_16x16x32_bf16 v[68:71], a[12:15], v[140:143], v[68:71]
buffer_load_dwordx4 v[204:207], v249, s[20:23], s92 offen offset:1024 sc1
v_mfma_f32_16x16x32_bf16 v[76:79], a[4:7], v[132:135], 0
v_mfma_f32_16x16x32_bf16 v[76:79], a[16:19], v[140:143], v[76:79]
global_load_dwordx4 a[252:255], v246, s[64:65]
v_mfma_f32_16x16x32_bf16 v[84:87], a[8:11], v[132:135], 0
s_waitcnt lgkmcnt(0)
s_barrier
s_cmp_eq_u32 s59, 1
s_cselect_b64 exec, -1, 0
ds_read_b128 a[240:243], v245 offset:8192
s_mov_b64 exec, -1
v_mfma_f32_16x16x32_bf16 v[84:87], a[20:23], v[140:143], v[84:87]
ds_read_b128 v[160:163], v1 offset:8192
v_mfma_f32_16x16x32_bf16 v[64:67], a[24:27], v[144:147], v[64:67]
ds_read_b128 v[164:167], v1 offset:9216
v_mfma_f32_16x16x32_bf16 v[64:67], a[36:39], v[152:155], v[64:67]
ds_read_b128 v[168:171], v1 offset:10240
v_mfma_f32_16x16x32_bf16 v[72:75], a[28:31], v[144:147], v[72:75]
ds_read_b128 v[172:175], v1 offset:11264
v_mfma_f32_16x16x32_bf16 v[72:75], a[40:43], v[152:155], v[72:75]
ds_read_b128 v[128:131], v1 offset:12288
v_mfma_f32_16x16x32_bf16 v[80:83], a[32:35], v[144:147], v[80:83]
ds_read_b128 v[132:135], v1 offset:13312
v_mfma_f32_16x16x32_bf16 v[80:83], a[44:47], v[152:155], v[80:83]
ds_read_b128 v[136:139], v1 offset:14336
v_mfma_f32_16x16x32_bf16 v[68:71], a[24:27], v[148:151], v[68:71]
ds_read_b128 v[140:143], v1 offset:15360
s_waitcnt vmcnt(11)
s_add_u32 s93, s93, 50176
v_mfma_f32_16x16x32_bf16 v[68:71], a[36:39], v[156:159], v[68:71]
buffer_load_dwordx4 v4, s[44:47], s93 offen offset:2048 lds
v_swap_b32 v210, v216
v_mfma_f32_16x16x32_bf16 v[76:79], a[28:31], v[148:151], v[76:79]
v_swap_b32 v211, v217
v_mfma_f32_16x16x32_bf16 v[76:79], a[40:43], v[156:159], v[76:79]
v_swap_b32 v214, v220
v_mfma_f32_16x16x32_bf16 v[84:87], a[32:35], v[148:151], v[84:87]
v_swap_b32 v215, v221
v_mfma_f32_16x16x32_bf16 v[84:87], a[44:47], v[156:159], v[84:87]
)A"
R"A(s_waitcnt lgkmcnt(0)
s_add_u32 s93, s93, 9216
v_mfma_f32_16x16x32_bf16 v[16:19], v[8:11], v[208:211], 0
buffer_load_dwordx4 v4, s[44:47], s93 offen offset:3072 lds
v_mfma_f32_16x16x32_bf16 v[20:23], v[8:11], v[216:219], 0
s_add_u32 m0, s56, 20480
s_add_u32 s93, s57, 368640
v_mfma_f32_16x16x32_bf16 v[24:27], v[8:11], v[212:215], 0
buffer_load_dwordx4 v4, s[44:47], s93 offen offset:0 lds
v_mfma_f32_16x16x32_bf16 v[28:31], v[8:11], v[220:223], 0
s_add_u32 s90, s83, 40960
s_add_u32 s92, s84, 40960
v_mfma_f32_16x16x32_bf16 v[64:67], a[48:51], v[160:163], v[64:67]
buffer_load_dwordx4 v[208:211], v247, s[20:23], s90 offen offset:0 sc1
v_mfma_f32_16x16x32_bf16 v[64:67], a[60:63], v[168:171], v[64:67]
v_cvt_pk_bf16_f32 v48, v16, v17
v_cvt_pk_bf16_f32 v49, v18, v19
v_mfma_f32_16x16x32_bf16 v[72:75], a[52:55], v[160:163], v[72:75]
v_cvt_pk_bf16_f32 v50, v20, v21
v_cvt_pk_bf16_f32 v51, v22, v23
v_mfma_f32_16x16x32_bf16 v[72:75], a[64:67], v[168:171], v[72:75]
ds_write_b128 v249, v[48:51] offset:0
v_mfma_f32_16x16x32_bf16 v[80:83], a[56:59], v[160:163], v[80:83]
v_cvt_pk_bf16_f32 v52, v24, v25
v_cvt_pk_bf16_f32 v53, v26, v27
v_mfma_f32_16x16x32_bf16 v[80:83], a[68:71], v[168:171], v[80:83]
v_cvt_pk_bf16_f32 v54, v28, v29
v_cvt_pk_bf16_f32 v55, v30, v31
v_mfma_f32_16x16x32_bf16 v[112:115], v[60:63], v[48:51], v[112:115]
ds_write_b128 v249, v[52:55] offset:1024
v_mfma_f32_16x16x32_bf16 v[120:123], v[48:51], v[48:51], v[120:123]
buffer_load_dwordx4 v[216:219], v247, s[20:23], s92 offen offset:0 sc1
v_mfma_f32_16x16x32_bf16 v[68:71], a[48:51], v[164:167], v[68:71]
v_mfma_f32_16x16x32_bf16 v[116:119], v[60:63], v[52:55], v[116:119]
buffer_load_dwordx4 v[212:215], v248, s[20:23], s90 offen offset:0 sc1
v_mfma_f32_16x16x32_bf16 v[124:127], v[52:55], v[52:55], v[124:127]
v_mfma_f32_16x16x32_bf16 v[68:71], a[60:63], v[172:175], v[68:71]
buffer_load_dwordx4 v[220:223], v248, s[20:23], s92 offen offset:0 sc1
v_mfma_f32_16x16x32_bf16 v[76:79], a[52:55], v[164:167], v[76:79]
v_mfma_f32_16x16x32_bf16 v[76:79], a[64:67], v[172:175], v[76:79]
global_load_dwordx2 v[6:7], v3, s[64:65]
v_mfma_f32_16x16x32_bf16 v[84:87], a[56:59], v[164:167], v[84:87]
s_waitcnt lgkmcnt(0)
s_barrier
s_cmp_eq_u32 s59, 2
s_cselect_b64 exec, -1, 0
ds_read_b128 a[240:243], v245 offset:0
s_mov_b64 exec, -1
v_mfma_f32_16x16x32_bf16 v[84:87], a[68:71], v[172:175], v[84:87]
ds_read_b128 v[144:147], v1 offset:0
v_mfma_f32_16x16x32_bf16 v[64:67], a[72:75], v[128:131], v[64:67]
ds_read_b128 v[148:151], v1 offset:1024
v_mfma_f32_16x16x32_bf16 v[64:67], a[84:87], v[136:139], v[64:67]
ds_read_b128 v[152:155], v1 offset:2048
v_mfma_f32_16x16x32_bf16 v[72:75], a[76:79], v[128:131], v[72:75]
ds_read_b128 v[156:159], v1 offset:3072
v_mfma_f32_16x16x32_bf16 v[72:75], a[88:91], v[136:139], v[72:75]
ds_read_b128 v[160:163], v1 offset:4096
v_mfma_f32_16x16x32_bf16 v[80:83], a[80:83], v[128:131], v[80:83]
ds_read_b128 v[164:167], v1 offset:5120
v_mfma_f32_16x16x32_bf16 v[80:83], a[92:95], v[136:139], v[80:83]
ds_read_b128 v[168:171], v1 offset:6144
v_mfma_f32_16x16x32_bf16 v[68:71], a[72:75], v[132:135], v[68:71]
ds_read_b128 v[172:175], v1 offset:7168
s_waitcnt vmcnt(15)
s_add_u32 s93, s93, 9216
v_mfma_f32_16x16x32_bf16 v[68:71], a[84:87], v[140:143], v[68:71]
buffer_load_dwordx4 v4, s[44:47], s93 offen offset:1024 lds
v_swap_b32 v178, v184
v_mfma_f32_16x16x32_bf16 v[76:79], a[76:79], v[132:135], v[76:79]
v_swap_b32 v179, v185
v_mfma_f32_16x16x32_bf16 v[76:79], a[88:91], v[140:143], v[76:79]
v_swap_b32 v182, v188
v_mfma_f32_16x16x32_bf16 v[84:87], a[80:83], v[132:135], v[84:87]
v_swap_b32 v183, v189
v_mfma_f32_16x16x32_bf16 v[84:87], a[92:95], v[140:143], v[84:87]
s_waitcnt lgkmcnt(0)
s_add_u32 s93, s93, 9216
v_mfma_f32_16x16x32_bf16 v[16:19], v[8:11], v[176:179], 0
buffer_load_dwordx4 v4, s[44:47], s93 offen offset:2048 lds
v_mfma_f32_16x16x32_bf16 v[20:23], v[8:11], v[184:187], 0
s_add_u32 s93, s93, 9216
v_mfma_f32_16x16x32_bf16 v[24:27], v[8:11], v[180:183], 0
buffer_load_dwordx4 v4, s[44:47], s93 offen offset:3072 lds
v_mfma_f32_16x16x32_bf16 v[28:31], v[8:11], v[188:191], 0
s_add_u32 s90, s83, 45056
s_add_u32 s92, s84, 45056
v_mfma_f32_16x16x32_bf16 v[64:67], a[96:99], v[144:147], v[64:67]
buffer_load_dwordx4 v[176:179], v247, s[20:23], s90 offen offset:0 sc1
v_mfma_f32_16x16x32_bf16 v[64:67], a[108:111], v[152:155], v[64:67]
v_cvt_pk_bf16_f32 v48, v16, v17
v_cvt_pk_bf16_f32 v49, v18, v19
v_mfma_f32_16x16x32_bf16 v[72:75], a[100:103], v[144:147], v[72:75]
v_cvt_pk_bf16_f32 v50, v20, v21
v_cvt_pk_bf16_f32 v51, v22, v23
v_mfma_f32_16x16x32_bf16 v[72:75], a[112:115], v[152:155], v[72:75]
ds_write_b128 v249, v[48:51] offset:8192
v_mfma_f32_16x16x32_bf16 v[80:83], a[104:107], v[144:147], v[80:83]
v_cvt_pk_bf16_f32 v52, v24, v25
v_cvt_pk_bf16_f32 v53, v26, v27
v_mfma_f32_16x16x32_bf16 v[80:83], a[116:119], v[152:155], v[80:83]
v_cvt_pk_bf16_f32 v54, v28, v29
v_cvt_pk_bf16_f32 v55, v30, v31
v_mfma_f32_16x16x32_bf16 v[112:115], v[60:63], v[48:51], v[112:115]
ds_write_b128 v249, v[52:55] offset:9216
v_mfma_f32_16x16x32_bf16 v[120:123], v[48:51], v[48:51], v[120:123]
buffer_load_dwordx4 v[184:187], v247, s[20:23], s92 offen offset:0 sc1
v_mfma_f32_16x16x32_bf16 v[68:71], a[96:99], v[148:151], v[68:71]
v_mfma_f32_16x16x32_bf16 v[116:119], v[60:63], v[52:55], v[116:119]
buffer_load_dwordx4 v[180:183], v248, s[20:23], s90 offen offset:0 sc1
v_mfma_f32_16x16x32_bf16 v[124:127], v[52:55], v[52:55], v[124:127]
v_mfma_f32_16x16x32_bf16 v[68:71], a[108:111], v[156:159], v[68:71]
buffer_load_dwordx4 v[188:191], v248, s[20:23], s92 offen offset:0 sc1
v_mfma_f32_16x16x32_bf16 v[76:79], a[100:103], v[148:151], v[76:79]
s_add_u32 m0, s56, 24576
s_add_u32 s93, s57, 430080
v_mfma_f32_16x16x32_bf16 v[76:79], a[112:115], v[156:159], v[76:79]
buffer_load_dwordx4 v4, s[44:47], s93 offen offset:0 lds
v_mfma_f32_16x16x32_bf16 v[84:87], a[104:107], v[148:151], v[84:87]
s_waitcnt lgkmcnt(0)
s_barrier
s_cmp_eq_u32 s59, 3
s_cselect_b64 exec, -1, 0
ds_read_b128 a[240:243], v245 offset:8192
s_mov_b64 exec, -1
v_mfma_f32_16x16x32_bf16 v[84:87], a[116:119], v[156:159], v[84:87]
ds_read_b128 v[128:131], v1 offset:8192
v_mfma_f32_16x16x32_bf16 v[64:67], a[120:123], v[160:163], v[64:67]
ds_read_b128 v[132:135], v1 offset:9216
v_mfma_f32_16x16x32_bf16 v[64:67], a[132:135], v[168:171], v[64:67]
ds_read_b128 v[136:139], v1 offset:10240
v_mfma_f32_16x16x32_bf16 v[72:75], a[124:127], v[160:163], v[72:75]
ds_read_b128 v[140:143], v1 offset:11264
v_mfma_f32_16x16x32_bf16 v[72:75], a[136:139], v[168:171], v[72:75]
ds_read_b128 v[144:147], v1 offset:12288
v_mfma_f32_16x16x32_bf16 v[80:83], a[128:131], v[160:163], v[80:83]
ds_read_b128 v[148:151], v1 offset:13312
v_mfma_f32_16x16x32_bf16 v[80:83], a[140:143], v[168:171], v[80:83]
ds_read_b128 v[152:155], v1 offset:14336
v_mfma_f32_16x16x32_bf16 v[68:71], a[120:123], v[164:167], v[68:71]
ds_read_b128 v[156:159], v1 offset:15360
s_waitcnt vmcnt(17)
v_mfma_f32_16x16x32_bf16 v[68:71], a[132:135], v[172:175], v[68:71]
v_swap_b32 v194, v200
v_mfma_f32_16x16x32_bf16 v[76:79], a[124:127], v[164:167], v[76:79]
v_swap_b32 v195, v201
v_mfma_f32_16x16x32_bf16 v[76:79], a[136:139], v[172:175], v[76:79]
v_swap_b32 v198, v204
v_mfma_f32_16x16x32_bf16 v[84:87], a[128:131], v[164:167], v[84:87]
v_swap_b32 v199, v205
v_mfma_f32_16x16x32_bf16 v[84:87], a[140:143], v[172:175], v[84:87]
s_waitcnt lgkmcnt(0)
s_add_u32 s93, s93, 9216
v_mfma_f32_16x16x32_bf16 v[16:19], v[8:11], v[192:195], 0
buffer_load_dwordx4 v4, s[44:47], s93 offen offset:1024 lds
v_mfma_f32_16x16x32_bf16 v[20:23], v[8:11], v[196:199], 0
s_add_u32 s93, s93, 9216
v_mfma_f32_16x16x32_bf16 v[24:27], v[8:11], v[200:203], 0
buffer_load_dwordx4 v4, s[44:47], s93 offen offset:2048 lds
v_mfma_f32_16x16x32_bf16 v[28:31], v[8:11], v[204:207], 0
s_add_u32 s90, s83, 49152
s_add_u32 s92, s84, 49152
v_mfma_f32_16x16x32_bf16 v[64:67], a[144:147], v[128:131], v[64:67]
buffer_load_dwordx4 v[192:195], v247, s[20:23], s90 offen offset:0 sc1
v_mfma_f32_16x16x32_bf16 v[64:67], a[156:159], v[136:139], v[64:67]
)A"
R"A(v_cvt_pk_bf16_f32 v48, v16, v17
v_cvt_pk_bf16_f32 v49, v18, v19
v_mfma_f32_16x16x32_bf16 v[72:75], a[148:151], v[128:131], v[72:75]
v_cvt_pk_bf16_f32 v50, v20, v21
v_cvt_pk_bf16_f32 v51, v22, v23
v_mfma_f32_16x16x32_bf16 v[72:75], a[160:163], v[136:139], v[72:75]
ds_write_b128 v249, v[48:51] offset:0
v_mfma_f32_16x16x32_bf16 v[80:83], a[152:155], v[128:131], v[80:83]
v_cvt_pk_bf16_f32 v52, v24, v25
v_cvt_pk_bf16_f32 v53, v26, v27
v_mfma_f32_16x16x32_bf16 v[80:83], a[164:167], v[136:139], v[80:83]
v_cvt_pk_bf16_f32 v54, v28, v29
v_cvt_pk_bf16_f32 v55, v30, v31
v_mfma_f32_16x16x32_bf16 v[112:115], v[60:63], v[48:51], v[112:115]
ds_write_b128 v249, v[52:55] offset:1024
v_mfma_f32_16x16x32_bf16 v[120:123], v[48:51], v[48:51], v[120:123]
buffer_load_dwordx4 v[200:203], v247, s[20:23], s92 offen offset:0 sc1
v_mfma_f32_16x16x32_bf16 v[68:71], a[144:147], v[132:135], v[68:71]
v_mfma_f32_16x16x32_bf16 v[116:119], v[60:63], v[52:55], v[116:119]
buffer_load_dwordx4 v[196:199], v248, s[20:23], s90 offen offset:0 sc1
v_mfma_f32_16x16x32_bf16 v[124:127], v[52:55], v[52:55], v[124:127]
v_mfma_f32_16x16x32_bf16 v[68:71], a[156:159], v[140:143], v[68:71]
buffer_load_dwordx4 v[204:207], v248, s[20:23], s92 offen offset:0 sc1
v_mfma_f32_16x16x32_bf16 v[76:79], a[148:151], v[132:135], v[76:79]
s_add_u32 s93, s93, 9216
v_mfma_f32_16x16x32_bf16 v[76:79], a[160:163], v[140:143], v[76:79]
buffer_load_dwordx4 v4, s[44:47], s93 offen offset:3072 lds
v_mfma_f32_16x16x32_bf16 v[84:87], a[152:155], v[132:135], v[84:87]
s_waitcnt lgkmcnt(0)
s_barrier
v_add_u32_e32 v16, s12, v1
ds_read_b64 a[244:245], v16 offset:0
v_mfma_f32_16x16x32_bf16 v[84:87], a[164:167], v[140:143], v[84:87]
ds_read_b128 v[160:163], v1 offset:0
v_mfma_f32_16x16x32_bf16 v[64:67], a[168:171], v[144:147], v[64:67]
ds_read_b128 v[164:167], v1 offset:1024
v_cndmask_b32_e64 v120, v120, v121, s[42:43]
v_cndmask_b32_e64 v122, v122, v123, s[42:43]
v_mfma_f32_16x16x32_bf16 v[64:67], a[180:183], v[152:155], v[64:67]
ds_read_b128 v[168:171], v1 offset:2048
v_cndmask_b32_e64 v113, v120, v122, s[52:53]
v_cndmask_b32_e64 v124, v124, v125, s[42:43]
v_mfma_f32_16x16x32_bf16 v[72:75], a[172:175], v[144:147], v[72:75]
ds_read_b128 v[172:175], v1 offset:3072
v_cndmask_b32_e64 v126, v126, v127, s[42:43]
v_cndmask_b32_e64 v117, v124, v126, s[52:53]
v_mfma_f32_16x16x32_bf16 v[72:75], a[184:187], v[152:155], v[72:75]
ds_read_b128 v[128:131], v1 offset:4096
v_mfma_f32_16x16x32_bf16 v[80:83], a[176:179], v[144:147], v[80:83]
ds_read_b128 v[132:135], v1 offset:5120
v_mfma_f32_16x16x32_bf16 v[80:83], a[188:191], v[152:155], v[80:83]
ds_read_b128 v[136:139], v1 offset:6144
s_mov_b64 exec, s[40:41]
ds_write_b64 v251, v[112:113] offset:16384
ds_write_b64 v251, v[116:117] offset:16896
s_mov_b64 exec, -1
v_mfma_f32_16x16x32_bf16 v[68:71], a[168:171], v[148:151], v[68:71]
ds_read_b128 v[140:143], v1 offset:7168
s_waitcnt vmcnt(16)
v_mfma_f32_16x16x32_bf16 v[68:71], a[180:183], v[156:159], v[68:71]
v_swap_b32 v210, v216
v_mfma_f32_16x16x32_bf16 v[76:79], a[172:175], v[148:151], v[76:79]
v_swap_b32 v211, v217
v_mfma_f32_16x16x32_bf16 v[76:79], a[184:187], v[156:159], v[76:79]
v_swap_b32 v214, v220
v_mfma_f32_16x16x32_bf16 v[84:87], a[176:179], v[148:151], v[84:87]
v_swap_b32 v215, v221
v_mfma_f32_16x16x32_bf16 v[84:87], a[188:191], v[156:159], v[84:87]
s_waitcnt lgkmcnt(0)
s_barrier
s_add_u32 m0, s56, 28672
s_add_u32 s93, s57, 409600
v_mfma_f32_16x16x32_bf16 v[16:19], v[8:11], v[208:211], 0
buffer_load_dwordx4 v4, s[44:47], s93 offen offset:0 lds
v_mfma_f32_16x16x32_bf16 v[20:23], v[8:11], v[216:219], 0
ds_read_b128 v[112:115], v253 offset:16384
ds_read_b128 v[116:119], v253 offset:16400
s_add_u32 s93, s93, 9216
v_mfma_f32_16x16x32_bf16 v[24:27], v[8:11], v[212:215], 0
buffer_load_dwordx4 v4, s[44:47], s93 offen offset:1024 lds
v_mfma_f32_16x16x32_bf16 v[28:31], v[8:11], v[220:223], 0
ds_read_b128 v[120:123], v253 offset:16896
ds_read_b128 v[124:127], v253 offset:16912
s_add_u32 s90, s83, 53248
s_add_u32 s92, s84, 53248
v_mfma_f32_16x16x32_bf16 v[64:67], a[192:195], v[160:163], v[64:67]
buffer_load_dwordx4 v[208:211], v247, s[20:23], s90 offen offset:0 sc1
v_mfma_f32_16x16x32_bf16 v[64:67], a[204:207], v[168:171], v[64:67]
v_cvt_pk_bf16_f32 v48, v16, v17
v_cvt_pk_bf16_f32 v49, v18, v19
v_mfma_f32_16x16x32_bf16 v[72:75], a[196:199], v[160:163], v[72:75]
v_cvt_pk_bf16_f32 v50, v20, v21
v_cvt_pk_bf16_f32 v51, v22, v23
v_mfma_f32_16x16x32_bf16 v[72:75], a[208:211], v[168:171], v[72:75]
ds_write_b128 v249, v[48:51] offset:8192
v_mfma_f32_16x16x32_bf16 v[80:83], a[200:203], v[160:163], v[80:83]
v_cvt_pk_bf16_f32 v52, v24, v25
v_cvt_pk_bf16_f32 v53, v26, v27
v_mfma_f32_16x16x32_bf16 v[80:83], a[212:215], v[168:171], v[80:83]
v_cvt_pk_bf16_f32 v54, v28, v29
v_cvt_pk_bf16_f32 v55, v30, v31
v_mfma_f32_16x16x32_bf16 v[224:227], v[60:63], v[48:51], 0
ds_write_b128 v249, v[52:55] offset:9216
v_mfma_f32_16x16x32_bf16 v[232:235], v[48:51], v[48:51], 0
buffer_load_dwordx4 v[216:219], v247, s[20:23], s92 offen offset:0 sc1
s_waitcnt lgkmcnt(2)
v_mfma_f32_16x16x32_bf16 v[68:71], a[192:195], v[164:167], v[68:71]
v_add_f32_e32 v112, v112, v114
v_add_f32_e32 v120, v120, v122
v_mfma_f32_16x16x32_bf16 v[228:231], v[60:63], v[52:55], 0
buffer_load_dwordx4 v[212:215], v248, s[20:23], s90 offen offset:0 sc1
v_mfma_f32_16x16x32_bf16 v[236:239], v[52:55], v[52:55], 0
v_add_f32_e32 v113, v113, v115
v_add_f32_e32 v121, v121, v123
v_mfma_f32_16x16x32_bf16 v[68:71], a[204:207], v[172:175], v[68:71]
buffer_load_dwordx4 v[220:223], v248, s[20:23], s92 offen offset:0 sc1
v_mfma_f32_16x16x32_bf16 v[76:79], a[196:199], v[164:167], v[76:79]
v_add_f32_e32 v116, v116, v118
v_add_f32_e32 v124, v124, v126
s_add_u32 s93, s93, 50176
v_mfma_f32_16x16x32_bf16 v[76:79], a[208:211], v[172:175], v[76:79]
buffer_load_dwordx4 v4, s[44:47], s93 offen offset:2048 lds
v_mfma_f32_16x16x32_bf16 v[84:87], a[200:203], v[164:167], v[84:87]
s_waitcnt lgkmcnt(0)
s_barrier
s_cmp_eq_u32 s59, 0
s_cselect_b64 exec, -1, 0
ds_read_b128 a[246:249], v245 offset:8192
s_mov_b64 exec, -1
v_mfma_f32_16x16x32_bf16 v[84:87], a[212:215], v[172:175], v[84:87]
ds_read_b128 v[144:147], v1 offset:8192
v_add_f32_e32 v117, v117, v119
v_add_f32_e32 v125, v125, v127
v_mfma_f32_16x16x32_bf16 v[64:67], a[216:219], v[128:131], v[64:67]
ds_read_b128 v[148:151], v1 offset:9216
v_add_f32_e32 v112, v112, v116
v_add_f32_e32 v120, v120, v124
v_mfma_f32_16x16x32_bf16 v[64:67], a[228:231], v[136:139], v[64:67]
ds_read_b128 v[152:155], v1 offset:10240
v_add_f32_e32 v113, v113, v117
v_add_f32_e32 v121, v121, v125
v_mfma_f32_16x16x32_bf16 v[72:75], a[220:223], v[128:131], v[72:75]
ds_read_b128 v[156:159], v1 offset:11264
v_mul_f32_e32 v112, s54, v112
v_mul_f32_e32 v120, s54, v120
v_mfma_f32_16x16x32_bf16 v[72:75], a[232:235], v[136:139], v[72:75]
ds_read_b128 v[160:163], v1 offset:12288
v_mul_f32_e32 v113, s54, v113
v_mul_f32_e32 v121, s54, v121
v_mfma_f32_16x16x32_bf16 v[80:83], a[224:227], v[128:131], v[80:83]
ds_read_b128 v[164:167], v1 offset:13312
v_fma_f32 v113, -v112, v112, v113
v_fma_f32 v121, -v120, v120, v121
v_mfma_f32_16x16x32_bf16 v[80:83], a[236:239], v[136:139], v[80:83]
ds_read_b128 v[168:171], v1 offset:14336
v_max_f32_e32 v113, 0, v113
v_max_f32_e32 v121, 0, v121
v_mfma_f32_16x16x32_bf16 v[68:71], a[216:219], v[132:135], v[68:71]
ds_read_b128 v[172:175], v1 offset:15360
s_waitcnt vmcnt(15)
v_add_f32_e32 v113, s55, v113
v_add_f32_e32 v121, s55, v121
v_mfma_f32_16x16x32_bf16 v[68:71], a[228:231], v[140:143], v[68:71]
v_rsq_f32_e32 v113, v113
v_rsq_f32_e32 v121, v121
v_swap_b32 v178, v184
v_mfma_f32_16x16x32_bf16 v[76:79], a[220:223], v[132:135], v[76:79]
v_swap_b32 v179, v185
v_mfma_f32_16x16x32_bf16 v[76:79], a[232:235], v[140:143], v[76:79]
v_swap_b32 v182, v188
v_mfma_f32_16x16x32_bf16 v[84:87], a[224:227], v[132:135], v[84:87]
v_swap_b32 v183, v189
v_mfma_f32_16x16x32_bf16 v[84:87], a[236:239], v[140:143], v[84:87]
s_waitcnt lgkmcnt(0)
v_pk_fma_f32 v[64:65], v[240:241], v[112:113], v[64:65] op_sel_hi:[1,0,1] neg_lo:[1,0,0] neg_hi:[1,0,0]
)A"
R"A(v_pk_fma_f32 v[66:67], v[242:243], v[112:113], v[66:67] op_sel_hi:[1,0,1] neg_lo:[1,0,0] neg_hi:[1,0,0]
v_pk_fma_f32 v[64:65], v[64:65], v[112:113], v[32:33] op_sel:[0,1,0] op_sel_hi:[1,1,1]
v_pk_fma_f32 v[66:67], v[66:67], v[112:113], v[34:35] op_sel:[0,1,0] op_sel_hi:[1,1,1]
v_max_f32_e32 v64, 0, v64
v_max_f32_e32 v65, 0, v65
v_max_f32_e32 v66, 0, v66
v_max_f32_e32 v67, 0, v67
v_pk_mul_f32 v[64:65], v[64:65], v[64:65]
v_pk_mul_f32 v[66:67], v[66:67], v[66:67]
v_cvt_pk_bf16_f32 v64, v64, v65
v_cvt_pk_bf16_f32 v65, v66, v67
v_pk_fma_f32 v[72:73], v[56:57], v[112:113], v[72:73] op_sel_hi:[1,0,1] neg_lo:[1,0,0] neg_hi:[1,0,0]
v_pk_fma_f32 v[74:75], v[58:59], v[112:113], v[74:75] op_sel_hi:[1,0,1] neg_lo:[1,0,0] neg_hi:[1,0,0]
v_pk_fma_f32 v[72:73], v[72:73], v[112:113], v[36:37] op_sel:[0,1,0] op_sel_hi:[1,1,1]
v_pk_fma_f32 v[74:75], v[74:75], v[112:113], v[38:39] op_sel:[0,1,0] op_sel_hi:[1,1,1]
v_max_f32_e32 v72, 0, v72
v_max_f32_e32 v73, 0, v73
v_max_f32_e32 v74, 0, v74
v_max_f32_e32 v75, 0, v75
v_pk_mul_f32 v[72:73], v[72:73], v[72:73]
v_pk_mul_f32 v[74:75], v[74:75], v[74:75]
v_cvt_pk_bf16_f32 v66, v72, v73
v_cvt_pk_bf16_f32 v67, v74, v75
buffer_store_dwordx4 v[64:67], v12, s[20:23], s28 offen offset:0 sc1
v_pk_fma_f32 v[80:81], v[44:45], v[112:113], v[80:81] op_sel_hi:[1,0,1] neg_lo:[1,0,0] neg_hi:[1,0,0]
v_pk_fma_f32 v[82:83], v[46:47], v[112:113], v[82:83] op_sel_hi:[1,0,1] neg_lo:[1,0,0] neg_hi:[1,0,0]
v_pk_fma_f32 v[80:81], v[80:81], v[112:113], v[40:41] op_sel:[0,1,0] op_sel_hi:[1,1,1]
v_pk_fma_f32 v[82:83], v[82:83], v[112:113], v[42:43] op_sel:[0,1,0] op_sel_hi:[1,1,1]
v_max_f32_e32 v80, 0, v80
v_max_f32_e32 v81, 0, v81
v_max_f32_e32 v82, 0, v82
v_max_f32_e32 v83, 0, v83
v_pk_mul_f32 v[80:81], v[80:81], v[80:81]
v_pk_mul_f32 v[82:83], v[82:83], v[82:83]
v_cvt_pk_bf16_f32 v80, v80, v81
v_cvt_pk_bf16_f32 v81, v82, v83
s_add_u32 s93, s93, 9216
v_mfma_f32_16x16x32_bf16 v[16:19], v[8:11], v[176:179], 0
buffer_load_dwordx4 v4, s[44:47], s93 offen offset:3072 lds
v_mfma_f32_16x16x32_bf16 v[20:23], v[8:11], v[184:187], 0
v_fma_f32 v68, -v240, v120, v68
v_fma_f32 v69, -v241, v120, v69
v_fma_f32 v70, -v242, v120, v70
v_mfma_f32_16x16x32_bf16 v[24:27], v[8:11], v[180:183], 0
v_fma_f32 v71, -v243, v120, v71
v_fma_f32 v68, v68, v121, v32
v_fma_f32 v69, v69, v121, v33
v_mfma_f32_16x16x32_bf16 v[28:31], v[8:11], v[188:191], 0
v_fma_f32 v70, v70, v121, v34
v_fma_f32 v71, v71, v121, v35
v_max_f32_e32 v68, 0, v68
s_add_u32 s90, s83, 73728
s_add_u32 s92, s84, 73728
v_mfma_f32_16x16x32_bf16 v[88:91], a[0:3], v[144:147], 0
buffer_load_dwordx4 v[176:179], v249, s[20:23], s90 offen offset:0 sc1
v_mfma_f32_16x16x32_bf16 v[88:91], a[12:15], v[152:155], v[88:91]
v_cvt_pk_bf16_f32 v48, v16, v17
v_cvt_pk_bf16_f32 v49, v18, v19
v_max_f32_e32 v69, 0, v69
v_mfma_f32_16x16x32_bf16 v[96:99], a[4:7], v[144:147], 0
v_cvt_pk_bf16_f32 v50, v20, v21
v_cvt_pk_bf16_f32 v51, v22, v23
v_max_f32_e32 v70, 0, v70
v_mfma_f32_16x16x32_bf16 v[96:99], a[16:19], v[152:155], v[96:99]
ds_write_b128 v249, v[48:51] offset:0
v_mfma_f32_16x16x32_bf16 v[104:107], a[8:11], v[144:147], 0
v_cvt_pk_bf16_f32 v52, v24, v25
v_cvt_pk_bf16_f32 v53, v26, v27
v_max_f32_e32 v71, 0, v71
v_mfma_f32_16x16x32_bf16 v[104:107], a[20:23], v[152:155], v[104:107]
v_cvt_pk_bf16_f32 v54, v28, v29
v_cvt_pk_bf16_f32 v55, v30, v31
v_mul_f32_e32 v68, v68, v68
v_mfma_f32_16x16x32_bf16 v[224:227], v[60:63], v[48:51], v[224:227]
ds_write_b128 v249, v[52:55] offset:1024
v_mfma_f32_16x16x32_bf16 v[232:235], v[48:51], v[48:51], v[232:235]
buffer_load_dwordx4 v[184:187], v249, s[20:23], s92 offen offset:0 sc1
v_mfma_f32_16x16x32_bf16 v[92:95], a[0:3], v[148:151], 0
v_mul_f32_e32 v69, v69, v69
v_mul_f32_e32 v70, v70, v70
v_mul_f32_e32 v71, v71, v71
v_mfma_f32_16x16x32_bf16 v[228:231], v[60:63], v[52:55], v[228:231]
buffer_load_dwordx4 v[180:183], v249, s[20:23], s90 offen offset:1024 sc1
v_mfma_f32_16x16x32_bf16 v[236:239], v[52:55], v[52:55], v[236:239]
v_cvt_pk_bf16_f32 v68, v68, v69
v_cvt_pk_bf16_f32 v69, v70, v71
v_fma_f32 v76, -v56, v120, v76
v_mfma_f32_16x16x32_bf16 v[92:95], a[12:15], v[156:159], v[92:95]
buffer_load_dwordx4 v[188:191], v249, s[20:23], s92 offen offset:1024 sc1
v_mfma_f32_16x16x32_bf16 v[100:103], a[4:7], v[148:151], 0
v_fma_f32 v77, -v57, v120, v77
v_fma_f32 v78, -v58, v120, v78
v_fma_f32 v79, -v59, v120, v79
v_mfma_f32_16x16x32_bf16 v[100:103], a[16:19], v[156:159], v[100:103]
v_fma_f32 v76, v76, v121, v36
v_fma_f32 v77, v77, v121, v37
v_fma_f32 v78, v78, v121, v38
v_mfma_f32_16x16x32_bf16 v[108:111], a[8:11], v[148:151], 0
s_waitcnt lgkmcnt(0)
s_barrier
s_cmp_eq_u32 s59, 1
s_cselect_b64 exec, -1, 0
ds_read_b128 a[246:249], v245 offset:0
s_mov_b64 exec, -1
v_fma_f32 v79, v79, v121, v39
v_max_f32_e32 v76, 0, v76
v_max_f32_e32 v77, 0, v77
v_mfma_f32_16x16x32_bf16 v[108:111], a[20:23], v[156:159], v[108:111]
ds_read_b128 v[128:131], v1 offset:0
v_max_f32_e32 v78, 0, v78
v_max_f32_e32 v79, 0, v79
v_mfma_f32_16x16x32_bf16 v[88:91], a[24:27], v[160:163], v[88:91]
ds_read_b128 v[132:135], v1 offset:1024
v_mul_f32_e32 v76, v76, v76
v_mul_f32_e32 v77, v77, v77
v_mfma_f32_16x16x32_bf16 v[88:91], a[36:39], v[168:171], v[88:91]
ds_read_b128 v[136:139], v1 offset:2048
v_mul_f32_e32 v78, v78, v78
v_mul_f32_e32 v79, v79, v79
v_mfma_f32_16x16x32_bf16 v[96:99], a[28:31], v[160:163], v[96:99]
ds_read_b128 v[140:143], v1 offset:3072
v_cvt_pk_bf16_f32 v70, v76, v77
v_cvt_pk_bf16_f32 v71, v78, v79
v_mfma_f32_16x16x32_bf16 v[96:99], a[40:43], v[168:171], v[96:99]
ds_read_b128 v[144:147], v1 offset:4096
buffer_store_dwordx4 v[68:71], v13, s[20:23], s28 offen offset:0 sc1
v_mfma_f32_16x16x32_bf16 v[104:107], a[32:35], v[160:163], v[104:107]
ds_read_b128 v[148:151], v1 offset:5120
v_fma_f32 v84, -v44, v120, v84
v_fma_f32 v85, -v45, v120, v85
v_mfma_f32_16x16x32_bf16 v[104:107], a[44:47], v[168:171], v[104:107]
ds_read_b128 v[152:155], v1 offset:6144
v_fma_f32 v86, -v46, v120, v86
v_fma_f32 v87, -v47, v120, v87
v_mfma_f32_16x16x32_bf16 v[92:95], a[24:27], v[164:167], v[92:95]
ds_read_b128 v[156:159], v1 offset:7168
s_waitcnt vmcnt(15)
v_fma_f32 v84, v84, v121, v40
v_fma_f32 v85, v85, v121, v41
v_mfma_f32_16x16x32_bf16 v[92:95], a[36:39], v[172:175], v[92:95]
v_fma_f32 v86, v86, v121, v42
v_fma_f32 v87, v87, v121, v43
v_max_f32_e32 v84, 0, v84
v_swap_b32 v194, v200
v_mfma_f32_16x16x32_bf16 v[100:103], a[28:31], v[164:167], v[100:103]
v_max_f32_e32 v85, 0, v85
v_max_f32_e32 v86, 0, v86
v_max_f32_e32 v87, 0, v87
v_swap_b32 v195, v201
v_mfma_f32_16x16x32_bf16 v[100:103], a[40:43], v[172:175], v[100:103]
v_mul_f32_e32 v84, v84, v84
v_mul_f32_e32 v85, v85, v85
v_mul_f32_e32 v86, v86, v86
v_swap_b32 v198, v204
v_mfma_f32_16x16x32_bf16 v[108:111], a[32:35], v[164:167], v[108:111]
v_mul_f32_e32 v87, v87, v87
v_cvt_pk_bf16_f32 v82, v84, v85
v_cvt_pk_bf16_f32 v83, v86, v87
v_swap_b32 v199, v205
v_mfma_f32_16x16x32_bf16 v[108:111], a[44:47], v[172:175], v[108:111]
s_waitcnt lgkmcnt(0)
s_cmp_eq_u32 s58, 0
s_cbranch_scc1 Lsw8_%=
v_swap_b32 v80, v82
v_swap_b32 v81, v83
Lsw8_%=:
buffer_store_dwordx4 v[80:83], v1, s[20:23], s28 offen offset:2048 sc1
s_add_u32 s91, s26, 0
v_mfma_f32_16x16x32_bf16 v[16:19], v[8:11], v[192:195], 0
buffer_load_dwordx4 a[0:3], v14, s[20:23], s91 offen offset:0 sc1
v_mfma_f32_16x16x32_bf16 v[20:23], v[8:11], v[200:203], 0
v_mfma_f32_16x16x32_bf16 v[24:27], v[8:11], v[196:199], 0
buffer_load_dwordx4 a[4:7], v14, s[20:23], s91 offen offset:1024 sc1
v_mfma_f32_16x16x32_bf16 v[28:31], v[8:11], v[204:207], 0
v_mfma_f32_16x16x32_bf16 v[88:91], a[48:51], v[128:131], v[88:91]
buffer_load_dwordx4 a[8:11], v15, s[20:23], s91 offen offset:0 sc1
v_mfma_f32_16x16x32_bf16 v[88:91], a[60:63], v[136:139], v[88:91]
v_cvt_pk_bf16_f32 v48, v16, v17
v_cvt_pk_bf16_f32 v49, v18, v19
v_mfma_f32_16x16x32_bf16 v[96:99], a[52:55], v[128:131], v[96:99]
v_cvt_pk_bf16_f32 v50, v20, v21
v_cvt_pk_bf16_f32 v51, v22, v23
v_mfma_f32_16x16x32_bf16 v[96:99], a[64:67], v[136:139], v[96:99]
ds_write_b128 v249, v[48:51] offset:8192
v_mfma_f32_16x16x32_bf16 v[104:107], a[56:59], v[128:131], v[104:107]
)A"
R"A(v_cvt_pk_bf16_f32 v52, v24, v25
v_cvt_pk_bf16_f32 v53, v26, v27
v_mfma_f32_16x16x32_bf16 v[104:107], a[68:71], v[136:139], v[104:107]
v_cvt_pk_bf16_f32 v54, v28, v29
v_cvt_pk_bf16_f32 v55, v30, v31
v_mfma_f32_16x16x32_bf16 v[224:227], v[60:63], v[48:51], v[224:227]
ds_write_b128 v249, v[52:55] offset:9216
v_mfma_f32_16x16x32_bf16 v[232:235], v[48:51], v[48:51], v[232:235]
buffer_load_dwordx4 a[12:15], v15, s[20:23], s91 offen offset:1024 sc1
v_mfma_f32_16x16x32_bf16 v[92:95], a[48:51], v[132:135], v[92:95]
v_mfma_f32_16x16x32_bf16 v[228:231], v[60:63], v[52:55], v[228:231]
buffer_load_dwordx4 a[16:19], v252, s[20:23], s91 offen offset:0 sc1
v_mfma_f32_16x16x32_bf16 v[236:239], v[52:55], v[52:55], v[236:239]
v_mfma_f32_16x16x32_bf16 v[92:95], a[60:63], v[140:143], v[92:95]
v_mfma_f32_16x16x32_bf16 v[100:103], a[52:55], v[132:135], v[100:103]
v_mfma_f32_16x16x32_bf16 v[100:103], a[64:67], v[140:143], v[100:103]
v_mfma_f32_16x16x32_bf16 v[108:111], a[56:59], v[132:135], v[108:111]
s_waitcnt lgkmcnt(0)
s_barrier
s_cmp_eq_u32 s59, 2
s_cselect_b64 exec, -1, 0
ds_read_b128 a[246:249], v245 offset:8192
s_mov_b64 exec, -1
v_mfma_f32_16x16x32_bf16 v[108:111], a[68:71], v[140:143], v[108:111]
ds_read_b128 v[160:163], v1 offset:8192
v_mfma_f32_16x16x32_bf16 v[88:91], a[72:75], v[144:147], v[88:91]
ds_read_b128 v[164:167], v1 offset:9216
v_mfma_f32_16x16x32_bf16 v[88:91], a[84:87], v[152:155], v[88:91]
ds_read_b128 v[168:171], v1 offset:10240
v_mfma_f32_16x16x32_bf16 v[96:99], a[76:79], v[144:147], v[96:99]
ds_read_b128 v[172:175], v1 offset:11264
v_mfma_f32_16x16x32_bf16 v[96:99], a[88:91], v[152:155], v[96:99]
ds_read_b128 v[128:131], v1 offset:12288
v_mfma_f32_16x16x32_bf16 v[104:107], a[80:83], v[144:147], v[104:107]
ds_read_b128 v[132:135], v1 offset:13312
v_mfma_f32_16x16x32_bf16 v[104:107], a[92:95], v[152:155], v[104:107]
ds_read_b128 v[136:139], v1 offset:14336
v_mfma_f32_16x16x32_bf16 v[92:95], a[72:75], v[148:151], v[92:95]
ds_read_b128 v[140:143], v1 offset:15360
s_waitcnt vmcnt(14)
v_mfma_f32_16x16x32_bf16 v[92:95], a[84:87], v[156:159], v[92:95]
v_swap_b32 v210, v216
v_mfma_f32_16x16x32_bf16 v[100:103], a[76:79], v[148:151], v[100:103]
v_swap_b32 v211, v217
v_mfma_f32_16x16x32_bf16 v[100:103], a[88:91], v[156:159], v[100:103]
v_swap_b32 v214, v220
v_mfma_f32_16x16x32_bf16 v[108:111], a[80:83], v[148:151], v[108:111]
v_swap_b32 v215, v221
v_mfma_f32_16x16x32_bf16 v[108:111], a[92:95], v[156:159], v[108:111]
s_waitcnt lgkmcnt(0) vmcnt(11)
v_xor_b32_e32 v4, 64, v5
s_add_u32 s91, s26, 5120
v_mfma_f32_16x16x32_bf16 v[16:19], v[8:11], v[208:211], 0
buffer_load_dwordx4 a[20:23], v14, s[20:23], s91 offen offset:0 sc1
v_mfma_f32_16x16x32_bf16 v[20:23], v[8:11], v[216:219], 0
ds_read_b64_tr_b16 a[80:81], v5 offset:0
v_mfma_f32_16x16x32_bf16 v[24:27], v[8:11], v[212:215], 0
buffer_load_dwordx4 a[24:27], v14, s[20:23], s91 offen offset:1024 sc1
v_mfma_f32_16x16x32_bf16 v[28:31], v[8:11], v[220:223], 0
ds_read_b64_tr_b16 a[82:83], v5 offset:2048
v_mfma_f32_16x16x32_bf16 v[88:91], a[96:99], v[160:163], v[88:91]
buffer_load_dwordx4 a[28:31], v15, s[20:23], s91 offen offset:0 sc1
v_mfma_f32_16x16x32_bf16 v[88:91], a[108:111], v[168:171], v[88:91]
ds_read_b64_tr_b16 a[84:85], v5 offset:8
v_cvt_pk_bf16_f32 v48, v16, v17
v_cvt_pk_bf16_f32 v49, v18, v19
v_mfma_f32_16x16x32_bf16 v[96:99], a[100:103], v[160:163], v[96:99]
ds_read_b64_tr_b16 a[86:87], v5 offset:2056
v_cvt_pk_bf16_f32 v50, v20, v21
v_cvt_pk_bf16_f32 v51, v22, v23
v_mfma_f32_16x16x32_bf16 v[96:99], a[112:115], v[168:171], v[96:99]
ds_write_b128 v249, v[48:51] offset:0
v_mfma_f32_16x16x32_bf16 v[104:107], a[104:107], v[160:163], v[104:107]
ds_read_b64_tr_b16 a[88:89], v4 offset:0
v_cvt_pk_bf16_f32 v52, v24, v25
v_cvt_pk_bf16_f32 v53, v26, v27
v_mfma_f32_16x16x32_bf16 v[104:107], a[116:119], v[168:171], v[104:107]
ds_read_b64_tr_b16 a[90:91], v4 offset:2048
v_cvt_pk_bf16_f32 v54, v28, v29
v_cvt_pk_bf16_f32 v55, v30, v31
v_mfma_f32_16x16x32_bf16 v[224:227], v[60:63], v[48:51], v[224:227]
ds_write_b128 v249, v[52:55] offset:1024
v_mfma_f32_16x16x32_bf16 v[232:235], v[48:51], v[48:51], v[232:235]
buffer_load_dwordx4 a[32:35], v15, s[20:23], s91 offen offset:1024 sc1
v_mfma_f32_16x16x32_bf16 v[92:95], a[96:99], v[164:167], v[92:95]
ds_read_b64_tr_b16 a[92:93], v4 offset:8
v_mfma_f32_16x16x32_bf16 v[228:231], v[60:63], v[52:55], v[228:231]
buffer_load_dwordx4 a[36:39], v252, s[20:23], s91 offen offset:0 sc1
v_mfma_f32_16x16x32_bf16 v[236:239], v[52:55], v[52:55], v[236:239]
v_mfma_f32_16x16x32_bf16 v[92:95], a[108:111], v[172:175], v[92:95]
v_mfma_f32_16x16x32_bf16 v[100:103], a[100:103], v[164:167], v[100:103]
v_mfma_f32_16x16x32_bf16 v[100:103], a[112:115], v[172:175], v[100:103]
v_mfma_f32_16x16x32_bf16 v[108:111], a[104:107], v[164:167], v[108:111]
s_waitcnt vmcnt(10) lgkmcnt(0)
s_barrier
s_cmp_lg_u32 s16, 0
s_cbranch_scc1 Lpb9_%=
v_mov_b32_e32 v18, s70
v_mov_b32_e32 v19, s71
s_mov_b64 exec, 1
buffer_store_dwordx2 v[18:19], off, s[20:23], s80 sc1
s_mov_b64 exec, -1
Lpb9_%=:
s_cmp_eq_u32 s59, 3
s_cselect_b64 exec, -1, 0
ds_read_b128 a[246:249], v245 offset:0
s_mov_b64 exec, -1
v_mfma_f32_16x16x32_bf16 v[108:111], a[116:119], v[172:175], v[108:111]
ds_read_b64_tr_b16 a[94:95], v4 offset:2056
ds_read_b128 v[144:147], v1 offset:0
v_mfma_f32_16x16x32_bf16 v[88:91], a[120:123], v[128:131], v[88:91]
ds_read_b128 v[148:151], v1 offset:1024
v_mfma_f32_16x16x32_bf16 v[88:91], a[132:135], v[136:139], v[88:91]
ds_read_b128 v[152:155], v1 offset:2048
v_mfma_f32_16x16x32_bf16 v[96:99], a[124:127], v[128:131], v[96:99]
ds_read_b128 v[156:159], v1 offset:3072
v_mfma_f32_16x16x32_bf16 v[96:99], a[136:139], v[136:139], v[96:99]
ds_read_b128 v[160:163], v1 offset:4096
v_mfma_f32_16x16x32_bf16 v[104:107], a[128:131], v[128:131], v[104:107]
ds_read_b128 v[164:167], v1 offset:5120
v_mfma_f32_16x16x32_bf16 v[104:107], a[140:143], v[136:139], v[104:107]
ds_read_b128 v[168:171], v1 offset:6144
v_mfma_f32_16x16x32_bf16 v[92:95], a[120:123], v[132:135], v[92:95]
ds_read_b128 v[172:175], v1 offset:7168
v_mfma_f32_16x16x32_bf16 v[92:95], a[132:135], v[140:143], v[92:95]
v_swap_b32 v178, v184
v_mfma_f32_16x16x32_bf16 v[100:103], a[124:127], v[132:135], v[100:103]
v_swap_b32 v179, v185
v_mfma_f32_16x16x32_bf16 v[100:103], a[136:139], v[140:143], v[100:103]
v_swap_b32 v182, v188
v_mfma_f32_16x16x32_bf16 v[108:111], a[128:131], v[132:135], v[108:111]
v_swap_b32 v183, v189
v_mfma_f32_16x16x32_bf16 v[108:111], a[140:143], v[140:143], v[108:111]
s_waitcnt lgkmcnt(0)
s_add_u32 s91, s26, 10240
v_mfma_f32_16x16x32_bf16 v[16:19], v[8:11], v[176:179], 0
buffer_load_dwordx4 a[40:43], v14, s[20:23], s91 offen offset:0 sc1
v_mfma_f32_16x16x32_bf16 v[20:23], v[8:11], v[180:183], 0
ds_read_b64_tr_b16 a[100:101], v5 offset:4096
v_mfma_f32_16x16x32_bf16 v[24:27], v[8:11], v[184:187], 0
buffer_load_dwordx4 a[44:47], v14, s[20:23], s91 offen offset:1024 sc1
v_mfma_f32_16x16x32_bf16 v[28:31], v[8:11], v[188:191], 0
ds_read_b64_tr_b16 a[102:103], v5 offset:6144
v_mfma_f32_16x16x32_bf16 v[88:91], a[144:147], v[144:147], v[88:91]
buffer_load_dwordx4 a[48:51], v15, s[20:23], s91 offen offset:0 sc1
v_mfma_f32_16x16x32_bf16 v[88:91], a[156:159], v[152:155], v[88:91]
ds_read_b64_tr_b16 a[104:105], v5 offset:4104
v_cvt_pk_bf16_f32 v48, v16, v17
v_cvt_pk_bf16_f32 v49, v18, v19
v_mfma_f32_16x16x32_bf16 v[96:99], a[148:151], v[144:147], v[96:99]
ds_read_b64_tr_b16 a[106:107], v5 offset:6152
v_cvt_pk_bf16_f32 v50, v20, v21
v_cvt_pk_bf16_f32 v51, v22, v23
v_mfma_f32_16x16x32_bf16 v[96:99], a[160:163], v[152:155], v[96:99]
ds_write_b128 v249, v[48:51] offset:8192
v_mfma_f32_16x16x32_bf16 v[104:107], a[152:155], v[144:147], v[104:107]
ds_read_b64_tr_b16 a[108:109], v4 offset:4096
v_cvt_pk_bf16_f32 v52, v24, v25
v_cvt_pk_bf16_f32 v53, v26, v27
v_mfma_f32_16x16x32_bf16 v[104:107], a[164:167], v[152:155], v[104:107]
ds_read_b64_tr_b16 a[110:111], v4 offset:6144
v_cvt_pk_bf16_f32 v54, v28, v29
v_cvt_pk_bf16_f32 v55, v30, v31
v_mfma_f32_16x16x32_bf16 v[224:227], v[60:63], v[48:51], v[224:227]
)A"
R"A(ds_write_b128 v249, v[52:55] offset:9216
v_mfma_f32_16x16x32_bf16 v[232:235], v[48:51], v[48:51], v[232:235]
buffer_load_dwordx4 a[52:55], v15, s[20:23], s91 offen offset:1024 sc1
v_mfma_f32_16x16x32_bf16 v[92:95], a[144:147], v[148:151], v[92:95]
ds_read_b64_tr_b16 a[112:113], v4 offset:4104
v_mfma_f32_16x16x32_bf16 v[228:231], v[60:63], v[52:55], v[228:231]
buffer_load_dwordx4 a[56:59], v252, s[20:23], s91 offen offset:0 sc1
v_mfma_f32_16x16x32_bf16 v[236:239], v[52:55], v[52:55], v[236:239]
v_mfma_f32_16x16x32_bf16 v[92:95], a[156:159], v[156:159], v[92:95]
v_mfma_f32_16x16x32_bf16 v[100:103], a[148:151], v[148:151], v[100:103]
v_mfma_f32_16x16x32_bf16 v[100:103], a[160:163], v[156:159], v[100:103]
v_mfma_f32_16x16x32_bf16 v[108:111], a[152:155], v[148:151], v[108:111]
s_waitcnt lgkmcnt(0)
s_barrier
v_add_u32_e32 v16, s12, v1
ds_read_b64 a[250:251], v16 offset:8192
v_mfma_f32_16x16x32_bf16 v[108:111], a[164:167], v[156:159], v[108:111]
ds_read_b64_tr_b16 a[114:115], v4 offset:6152
ds_read_b128 v[128:131], v1 offset:8192
v_mfma_f32_16x16x32_bf16 v[88:91], a[168:171], v[160:163], v[88:91]
ds_read_b64_tr_b16 a[120:121], v5 offset:8192
ds_read_b128 v[136:139], v1 offset:10240
v_cndmask_b32_e64 v232, v232, v233, s[42:43]
v_cndmask_b32_e64 v234, v234, v235, s[42:43]
v_mfma_f32_16x16x32_bf16 v[88:91], a[180:183], v[168:171], v[88:91]
ds_read_b64_tr_b16 a[122:123], v5 offset:10240
ds_read_b128 v[144:147], v1 offset:12288
v_cndmask_b32_e64 v225, v232, v234, s[52:53]
v_cndmask_b32_e64 v236, v236, v237, s[42:43]
v_mfma_f32_16x16x32_bf16 v[96:99], a[172:175], v[160:163], v[96:99]
ds_read_b64_tr_b16 a[124:125], v5 offset:8200
ds_read_b128 v[152:155], v1 offset:14336
v_cndmask_b32_e64 v238, v238, v239, s[42:43]
v_cndmask_b32_e64 v229, v236, v238, s[52:53]
v_mfma_f32_16x16x32_bf16 v[96:99], a[184:187], v[168:171], v[96:99]
ds_read_b64_tr_b16 a[126:127], v5 offset:10248
ds_read_b128 v[132:135], v1 offset:9216
v_mfma_f32_16x16x32_bf16 v[104:107], a[176:179], v[160:163], v[104:107]
ds_read_b64_tr_b16 a[128:129], v4 offset:8192
ds_read_b128 v[140:143], v1 offset:11264
v_mfma_f32_16x16x32_bf16 v[104:107], a[188:191], v[168:171], v[104:107]
ds_read_b128 v[148:151], v1 offset:13312
s_mov_b64 exec, s[40:41]
ds_write_b64 v251, v[224:225] offset:17408
ds_write_b64 v251, v[228:229] offset:17920
s_mov_b64 exec, -1
v_mfma_f32_16x16x32_bf16 v[92:95], a[168:171], v[164:167], v[92:95]
ds_read_b128 v[156:159], v1 offset:15360
v_mfma_f32_16x16x32_bf16 v[92:95], a[180:183], v[172:175], v[92:95]
v_mfma_f32_16x16x32_bf16 v[100:103], a[172:175], v[164:167], v[100:103]
v_mfma_f32_16x16x32_bf16 v[100:103], a[184:187], v[172:175], v[100:103]
v_mfma_f32_16x16x32_bf16 v[108:111], a[176:179], v[164:167], v[108:111]
v_mfma_f32_16x16x32_bf16 v[108:111], a[188:191], v[172:175], v[108:111]
s_waitcnt lgkmcnt(0)
s_barrier
v_mfma_f32_16x16x32_bf16 v[88:91], a[192:195], v[128:131], v[88:91]
ds_read_b128 v[224:227], v253 offset:17408
buffer_load_dwordx2 v[244:245], v250, s[20:23], s81 offen sc1
s_add_u32 s91, s26, 15360
v_mfma_f32_16x16x32_bf16 v[88:91], a[204:207], v[136:139], v[88:91]
ds_read_b128 v[228:231], v253 offset:17424
buffer_load_dwordx4 a[60:63], v14, s[20:23], s91 offen offset:0 sc1
v_mfma_f32_16x16x32_bf16 v[88:91], a[216:219], v[144:147], v[88:91]
ds_read_b128 v[232:235], v253 offset:17920
v_mfma_f32_16x16x32_bf16 v[88:91], a[228:231], v[152:155], v[88:91]
ds_read_b128 v[236:239], v253 offset:17936
buffer_load_dwordx4 a[64:67], v14, s[20:23], s91 offen offset:1024 sc1
v_mfma_f32_16x16x32_bf16 v[96:99], a[196:199], v[128:131], v[96:99]
v_mfma_f32_16x16x32_bf16 v[96:99], a[208:211], v[136:139], v[96:99]
ds_read_b64_tr_b16 a[130:131], v4 offset:10240
s_waitcnt lgkmcnt(4)
v_add_f32_e32 v224, v224, v226
s_waitcnt lgkmcnt(2)
v_add_f32_e32 v232, v232, v234
buffer_load_dwordx4 a[68:71], v15, s[20:23], s91 offen offset:0 sc1
v_mfma_f32_16x16x32_bf16 v[96:99], a[220:223], v[144:147], v[96:99]
ds_read_b64_tr_b16 a[132:133], v4 offset:8200
v_add_f32_e32 v225, v225, v227
v_add_f32_e32 v233, v233, v235
v_mfma_f32_16x16x32_bf16 v[96:99], a[232:235], v[152:155], v[96:99]
ds_read_b64_tr_b16 a[134:135], v4 offset:10248
v_add_f32_e32 v228, v228, v230
s_waitcnt lgkmcnt(3)
v_add_f32_e32 v236, v236, v238
buffer_load_dwordx4 a[72:75], v15, s[20:23], s91 offen offset:1024 sc1
v_mfma_f32_16x16x32_bf16 v[104:107], a[200:203], v[128:131], v[104:107]
ds_read_b64_tr_b16 a[140:141], v5 offset:12288
v_add_f32_e32 v229, v229, v231
v_add_f32_e32 v237, v237, v239
v_mfma_f32_16x16x32_bf16 v[104:107], a[212:215], v[136:139], v[104:107]
ds_read_b64_tr_b16 a[142:143], v5 offset:14336
v_add_f32_e32 v224, v224, v228
v_add_f32_e32 v232, v232, v236
buffer_load_dwordx4 a[76:79], v252, s[20:23], s91 offen offset:0 sc1
v_mfma_f32_16x16x32_bf16 v[104:107], a[224:227], v[144:147], v[104:107]
ds_read_b64_tr_b16 a[144:145], v5 offset:12296
v_add_f32_e32 v225, v225, v229
v_add_f32_e32 v233, v233, v237
v_mfma_f32_16x16x32_bf16 v[104:107], a[236:239], v[152:155], v[104:107]
ds_read_b64_tr_b16 a[146:147], v5 offset:14344
v_mul_f32_e32 v224, s54, v224
v_mul_f32_e32 v232, s54, v232
v_mfma_f32_16x16x32_bf16 v[92:95], a[192:195], v[132:135], v[92:95]
ds_read_b64_tr_b16 a[148:149], v4 offset:12288
v_mul_f32_e32 v225, s54, v225
v_mul_f32_e32 v233, s54, v233
v_mfma_f32_16x16x32_bf16 v[92:95], a[204:207], v[140:143], v[92:95]
ds_read_b64_tr_b16 a[150:151], v4 offset:14336
v_fma_f32 v225, -v224, v224, v225
v_fma_f32 v233, -v232, v232, v233
v_mfma_f32_16x16x32_bf16 v[92:95], a[216:219], v[148:151], v[92:95]
ds_read_b64_tr_b16 a[152:153], v4 offset:12296
v_max_f32_e32 v225, 0, v225
v_max_f32_e32 v233, 0, v233
v_mfma_f32_16x16x32_bf16 v[92:95], a[228:231], v[156:159], v[92:95]
ds_read_b64_tr_b16 a[154:155], v4 offset:14344
v_add_f32_e32 v225, s55, v225
v_add_f32_e32 v233, s55, v233
v_mfma_f32_16x16x32_bf16 v[100:103], a[196:199], v[132:135], v[100:103]
ds_read_b64_tr_b16 a[160:161], v5 offset:16384
v_rsq_f32_e32 v225, v225
v_rsq_f32_e32 v233, v233
v_mfma_f32_16x16x32_bf16 v[100:103], a[208:211], v[140:143], v[100:103]
ds_read_b64_tr_b16 a[162:163], v5 offset:18432
v_fma_f32 v88, -v240, v224, v88
v_mfma_f32_16x16x32_bf16 v[100:103], a[220:223], v[148:151], v[100:103]
ds_read_b64_tr_b16 a[164:165], v5 offset:16392
v_fma_f32 v89, -v241, v224, v89
v_mfma_f32_16x16x32_bf16 v[100:103], a[232:235], v[156:159], v[100:103]
ds_read_b64_tr_b16 a[166:167], v5 offset:18440
v_fma_f32 v90, -v242, v224, v90
v_mfma_f32_16x16x32_bf16 v[108:111], a[200:203], v[132:135], v[108:111]
ds_read_b64_tr_b16 a[168:169], v4 offset:16384
v_fma_f32 v91, -v243, v224, v91
v_mfma_f32_16x16x32_bf16 v[108:111], a[212:215], v[140:143], v[108:111]
ds_read_b64_tr_b16 a[170:171], v4 offset:18432
v_fma_f32 v88, v88, v225, v32
v_mfma_f32_16x16x32_bf16 v[108:111], a[224:227], v[148:151], v[108:111]
ds_read_b64_tr_b16 a[172:173], v4 offset:16392
v_fma_f32 v89, v89, v225, v33
v_mfma_f32_16x16x32_bf16 v[108:111], a[236:239], v[156:159], v[108:111]
ds_read_b64_tr_b16 a[174:175], v4 offset:18440
v_fma_f32 v90, v90, v225, v34
v_fma_f32 v91, v91, v225, v35
v_max_f32_e32 v88, 0, v88
v_max_f32_e32 v89, 0, v89
v_max_f32_e32 v90, 0, v90
v_max_f32_e32 v91, 0, v91
v_mul_f32_e32 v88, v88, v88
v_mul_f32_e32 v89, v89, v89
v_mul_f32_e32 v90, v90, v90
v_mul_f32_e32 v91, v91, v91
v_cvt_pk_bf16_f32 v88, v88, v89
v_cvt_pk_bf16_f32 v89, v90, v91
s_waitcnt vmcnt(5)
v_cmp_ne_u64_e32 vcc, s[70:71], v[244:245]
s_mov_b32 s8, 0x200000
s_cmp_eq_u64 vcc, 0
s_cbranch_scc1 Lfb10_%=
Lfs10_%=:
buffer_load_dwordx2 v[244:245], v250, s[20:23], s81 offen sc1
s_waitcnt vmcnt(0)
v_cmp_ne_u64_e32 vcc, s[70:71], v[244:245]
s_cmp_eq_u64 vcc, 0
s_cbranch_scc1 Lfb10_%=
s_sub_u32 s8, s8, 1
s_cbranch_scc0 Lfs10_%=
Lfb10_%=:
s_add_u32 s90, s85, 0
buffer_load_dwordx4 v[176:179], v1, s[20:23], s90 offen offset:0 sc1
buffer_load_dwordx4 v[180:183], v1, s[20:23], s90 offen offset:1024 sc1
buffer_load_dwordx4 v[184:187], v1, s[20:23], s90 offen offset:3072 sc1
buffer_load_dwordx4 v[188:191], v252, s[20:23], s90 offen offset:0 sc1
buffer_load_dwordx4 v[192:195], v1, s[20:23], s90 offen offset:2048 sc1
)A"
R"A(buffer_load_dwordx4 v[196:199], v252, s[20:23], s90 offen offset:1024 sc1
v_pk_fma_f32 v[96:97], v[56:57], v[224:225], v[96:97] op_sel_hi:[1,0,1] neg_lo:[1,0,0] neg_hi:[1,0,0]
v_pk_fma_f32 v[98:99], v[58:59], v[224:225], v[98:99] op_sel_hi:[1,0,1] neg_lo:[1,0,0] neg_hi:[1,0,0]
v_pk_fma_f32 v[96:97], v[96:97], v[224:225], v[36:37] op_sel:[0,1,0] op_sel_hi:[1,1,1]
v_pk_fma_f32 v[98:99], v[98:99], v[224:225], v[38:39] op_sel:[0,1,0] op_sel_hi:[1,1,1]
v_max_f32_e32 v96, 0, v96
v_max_f32_e32 v97, 0, v97
v_max_f32_e32 v98, 0, v98
v_max_f32_e32 v99, 0, v99
v_pk_mul_f32 v[96:97], v[96:97], v[96:97]
v_pk_mul_f32 v[98:99], v[98:99], v[98:99]
v_cvt_pk_bf16_f32 v90, v96, v97
v_cvt_pk_bf16_f32 v91, v98, v99
buffer_store_dwordx4 v[88:91], v12, s[20:23], s29 offen offset:0 sc1
v_pk_fma_f32 v[104:105], v[44:45], v[224:225], v[104:105] op_sel_hi:[1,0,1] neg_lo:[1,0,0] neg_hi:[1,0,0]
v_pk_fma_f32 v[106:107], v[46:47], v[224:225], v[106:107] op_sel_hi:[1,0,1] neg_lo:[1,0,0] neg_hi:[1,0,0]
v_pk_fma_f32 v[104:105], v[104:105], v[224:225], v[40:41] op_sel:[0,1,0] op_sel_hi:[1,1,1]
v_pk_fma_f32 v[106:107], v[106:107], v[224:225], v[42:43] op_sel:[0,1,0] op_sel_hi:[1,1,1]
v_max_f32_e32 v104, 0, v104
v_max_f32_e32 v105, 0, v105
v_max_f32_e32 v106, 0, v106
v_max_f32_e32 v107, 0, v107
v_pk_mul_f32 v[104:105], v[104:105], v[104:105]
v_pk_mul_f32 v[106:107], v[106:107], v[106:107]
v_cvt_pk_bf16_f32 v104, v104, v105
v_cvt_pk_bf16_f32 v105, v106, v107
v_pk_fma_f32 v[92:93], v[240:241], v[232:233], v[92:93] op_sel_hi:[1,0,1] neg_lo:[1,0,0] neg_hi:[1,0,0]
v_pk_fma_f32 v[94:95], v[242:243], v[232:233], v[94:95] op_sel_hi:[1,0,1] neg_lo:[1,0,0] neg_hi:[1,0,0]
v_pk_fma_f32 v[92:93], v[92:93], v[232:233], v[32:33] op_sel:[0,1,0] op_sel_hi:[1,1,1]
v_pk_fma_f32 v[94:95], v[94:95], v[232:233], v[34:35] op_sel:[0,1,0] op_sel_hi:[1,1,1]
v_max_f32_e32 v92, 0, v92
v_max_f32_e32 v93, 0, v93
v_max_f32_e32 v94, 0, v94
v_max_f32_e32 v95, 0, v95
v_pk_mul_f32 v[92:93], v[92:93], v[92:93]
v_pk_mul_f32 v[94:95], v[94:95], v[94:95]
v_cvt_pk_bf16_f32 v92, v92, v93
v_cvt_pk_bf16_f32 v93, v94, v95
v_pk_fma_f32 v[100:101], v[56:57], v[232:233], v[100:101] op_sel_hi:[1,0,1] neg_lo:[1,0,0] neg_hi:[1,0,0]
v_pk_fma_f32 v[102:103], v[58:59], v[232:233], v[102:103] op_sel_hi:[1,0,1] neg_lo:[1,0,0] neg_hi:[1,0,0]
v_pk_fma_f32 v[100:101], v[100:101], v[232:233], v[36:37] op_sel:[0,1,0] op_sel_hi:[1,1,1]
v_pk_fma_f32 v[102:103], v[102:103], v[232:233], v[38:39] op_sel:[0,1,0] op_sel_hi:[1,1,1]
v_max_f32_e32 v100, 0, v100
v_max_f32_e32 v101, 0, v101
v_max_f32_e32 v102, 0, v102
v_max_f32_e32 v103, 0, v103
v_pk_mul_f32 v[100:101], v[100:101], v[100:101]
v_pk_mul_f32 v[102:103], v[102:103], v[102:103]
v_cvt_pk_bf16_f32 v94, v100, v101
v_cvt_pk_bf16_f32 v95, v102, v103
buffer_store_dwordx4 v[92:95], v13, s[20:23], s29 offen offset:0 sc1
v_pk_fma_f32 v[108:109], v[44:45], v[232:233], v[108:109] op_sel_hi:[1,0,1] neg_lo:[1,0,0] neg_hi:[1,0,0]
v_pk_fma_f32 v[110:111], v[46:47], v[232:233], v[110:111] op_sel_hi:[1,0,1] neg_lo:[1,0,0] neg_hi:[1,0,0]
v_pk_fma_f32 v[108:109], v[108:109], v[232:233], v[40:41] op_sel:[0,1,0] op_sel_hi:[1,1,1]
v_pk_fma_f32 v[110:111], v[110:111], v[232:233], v[42:43] op_sel:[0,1,0] op_sel_hi:[1,1,1]
v_max_f32_e32 v108, 0, v108
v_max_f32_e32 v109, 0, v109
v_max_f32_e32 v110, 0, v110
v_max_f32_e32 v111, 0, v111
v_pk_mul_f32 v[108:109], v[108:109], v[108:109]
v_pk_mul_f32 v[110:111], v[110:111], v[110:111]
v_cvt_pk_bf16_f32 v106, v108, v109
v_cvt_pk_bf16_f32 v107, v110, v111
s_cmp_eq_u32 s58, 0
s_cbranch_scc1 Lsw11_%=
v_swap_b32 v104, v106
v_swap_b32 v105, v107
Lsw11_%=:
buffer_store_dwordx4 v[104:107], v1, s[20:23], s29 offen offset:2048 sc1
s_add_u32 s81, s81, 64
s_add_u32 s90, s85, 6144
buffer_load_dwordx4 v[200:203], v1, s[20:23], s90 offen offset:0 sc1
buffer_load_dwordx4 v[204:207], v1, s[20:23], s90 offen offset:1024 sc1
buffer_load_dwordx4 v[208:211], v1, s[20:23], s90 offen offset:3072 sc1
buffer_load_dwordx4 v[212:215], v252, s[20:23], s90 offen offset:0 sc1
ds_read_b64_tr_b16 a[180:181], v5 offset:20480
ds_read_b64_tr_b16 a[182:183], v5 offset:22528
ds_read_b64_tr_b16 a[184:185], v5 offset:20488
ds_read_b64_tr_b16 a[186:187], v5 offset:22536
ds_read_b64_tr_b16 a[188:189], v4 offset:20480
ds_read_b64_tr_b16 a[190:191], v4 offset:22528
ds_read_b64_tr_b16 a[192:193], v4 offset:20488
ds_read_b64_tr_b16 a[194:195], v4 offset:22536
ds_read_b64_tr_b16 a[200:201], v5 offset:24576
s_add_u32 s91, s26, 20480
buffer_load_dwordx4 a[96:99], v252, s[20:23], s91 offen offset:0 sc1
ds_read_b64_tr_b16 a[202:203], v5 offset:26624
ds_read_b64_tr_b16 a[204:205], v5 offset:24584
ds_read_b64_tr_b16 a[206:207], v5 offset:26632
s_sub_u32 s88, s75, 1
s_and_b32 s89, s88, s98
v_accvgpr_read_b32 v24, a252
v_accvgpr_read_b32 v25, a253
v_accvgpr_read_b32 v26, a254
v_accvgpr_read_b32 v27, a255
buffer_load_dwordx4 v[216:219], v1, s[20:23], s90 offen offset:2048 sc1
v_lshlrev_b32_e32 v100, 16, v24
v_and_b32_e32 v101, s98, v24
v_lshlrev_b32_e32 v102, 16, v25
v_and_b32_e32 v103, s98, v25
v_lshlrev_b32_e32 v104, 16, v26
v_and_b32_e32 v105, s98, v26
v_lshlrev_b32_e32 v106, 16, v27
v_and_b32_e32 v107, s98, v27
v_lshlrev_b32_e32 v116, 16, v6
buffer_load_dwordx4 v[220:223], v252, s[20:23], s90 offen offset:1024 sc1
v_and_b32_e32 v117, s89, v6
v_and_b32_e32 v116, s88, v116
v_lshlrev_b32_e32 v118, 16, v7
v_and_b32_e32 v119, s89, v7
v_and_b32_e32 v118, s88, v118
v_mov_b32_e32 v10, 0
v_mov_b32_e32 v11, 0
v_mov_b32_e32 v12, v8
v_mov_b32_e32 v13, v9
s_add_u32 s91, s26, 25600
buffer_load_dwordx4 a[116:119], v252, s[20:23], s91 offen offset:0 sc1
v_mov_b32_e32 v28, 0
v_mov_b32_e32 v29, 0
v_and_b32_e32 v30, s88, v8
v_and_b32_e32 v31, s88, v9
v_mfma_f32_16x16x32_bf16 v[60:63], v[8:11], a[240:243], v[100:103]
v_mfma_f32_16x16x32_bf16 v[64:67], v[10:13], a[240:243], v[104:107]
v_mfma_f32_16x16x32_bf16 v[76:79], v[28:31], a[242:245], v[116:119]
v_mfma_f32_16x16x32_bf16 v[100:103], v[8:11], a[246:249], v[100:103]
v_mfma_f32_16x16x32_bf16 v[104:107], v[10:13], a[246:249], v[104:107]
s_add_u32 s90, s85, 12288
buffer_load_dwordx4 v[224:227], v1, s[20:23], s90 offen offset:0 sc1
v_mfma_f32_16x16x32_bf16 v[116:119], v[28:31], a[248:251], v[116:119]
s_movk_i32 s88, 1280
v_mad_u32_u24 v16, v2, s88, v246
v_mad_u32_u24 v17, v2, s88, v3
s_lshl_b32 s88, s75, 30
v_add_u32_e32 v17, s88, v17
s_xor_b32 s88, s16, 0
s_lshl_b32 s88, s88, 13
v_add_u32_e32 v20, s88, v1
buffer_load_dwordx4 v[228:231], v1, s[20:23], s90 offen offset:1024 sc1
s_xor_b32 s88, s16, 1
s_lshl_b32 s88, s88, 13
v_add_u32_e32 v21, s88, v1
s_xor_b32 s88, s16, 2
s_lshl_b32 s88, s88, 13
v_add_u32_e32 v22, s88, v1
s_xor_b32 s88, s16, 3
s_lshl_b32 s88, s88, 13
v_add_u32_e32 v23, s88, v1
s_add_u32 s80, s80, 64
s_waitcnt vmcnt(10) lgkmcnt(0)
s_barrier
s_cmp_lg_u32 s16, 0
s_cbranch_scc1 Lpb12_%=
v_mov_b32_e32 v58, s70
v_mov_b32_e32 v59, s71
s_mov_b64 exec, 1
buffer_store_dwordx2 v[58:59], off, s[20:23], s80 sc1
s_mov_b64 exec, -1
Lpb12_%=:
s_add_u32 s91, s26, 30720
v_mfma_f32_16x16x32_bf16 v[80:83], a[0:3], v[180:183], 0
buffer_load_dwordx4 a[136:139], v252, s[20:23], s91 offen offset:0 sc1
v_mfma_f32_16x16x32_bf16 v[84:87], a[4:7], v[180:183], 0
ds_read_b64_tr_b16 a[208:209], v4 offset:24576
ds_read_b64_tr_b16 a[210:211], v4 offset:26624
v_mfma_f32_16x16x32_bf16 v[88:91], a[8:11], v[180:183], 0
buffer_load_dwordx4 v[232:235], v1, s[20:23], s90 offen offset:3072 sc1
v_mfma_f32_16x16x32_bf16 v[92:95], a[12:15], v[180:183], 0
ds_read_b64_tr_b16 a[212:213], v4 offset:24584
ds_read_b64_tr_b16 a[214:215], v4 offset:26632
v_mfma_f32_16x16x32_bf16 v[96:99], a[16:19], v[180:183], 0
buffer_load_dwordx4 v[140:143], v252, s[20:23], s90 offen offset:0 sc1
v_mfma_f32_16x16x32_bf16 v[60:63], a[0:3], v[176:179], v[60:63]
ds_read_b64_tr_b16 a[220:221], v5 offset:28672
ds_read_b64_tr_b16 a[222:223], v5 offset:30720
s_add_u32 s91, s26, 35840
v_mfma_f32_16x16x32_bf16 v[64:67], a[4:7], v[176:179], v[64:67]
buffer_load_dwordx4 a[156:159], v252, s[20:23], s91 offen offset:0 sc1
v_mfma_f32_16x16x32_bf16 v[68:71], a[8:11], v[176:179], 0
)A"
R"A(ds_read_b64_tr_b16 a[224:225], v5 offset:28680
ds_read_b64_tr_b16 a[226:227], v5 offset:30728
v_mfma_f32_16x16x32_bf16 v[72:75], a[12:15], v[176:179], 0
ds_read_b64_tr_b16 a[228:229], v4 offset:28672
ds_read_b64_tr_b16 a[230:231], v4 offset:30720
v_mfma_f32_16x16x32_bf16 v[76:79], a[16:19], v[176:179], v[76:79]
ds_read_b64_tr_b16 a[232:233], v4 offset:28680
ds_read_b64_tr_b16 a[234:235], v4 offset:30728
v_mfma_f32_16x16x32_bf16 v[80:83], a[20:23], v[188:191], v[80:83]
buffer_load_dwordx4 v[144:147], v1, s[20:23], s90 offen offset:2048 sc1
v_mfma_f32_16x16x32_bf16 v[84:87], a[24:27], v[188:191], v[84:87]
v_mfma_f32_16x16x32_bf16 v[88:91], a[28:31], v[188:191], v[88:91]
buffer_load_dwordx4 v[148:151], v252, s[20:23], s90 offen offset:1024 sc1
v_mfma_f32_16x16x32_bf16 v[92:95], a[32:35], v[188:191], v[92:95]
s_add_u32 s91, s26, 40960
v_mfma_f32_16x16x32_bf16 v[96:99], a[36:39], v[188:191], v[96:99]
buffer_load_dwordx4 a[176:179], v252, s[20:23], s91 offen offset:0 sc1
v_mfma_f32_16x16x32_bf16 v[60:63], a[20:23], v[184:187], v[60:63]
s_add_u32 s90, s85, 18432
v_mfma_f32_16x16x32_bf16 v[64:67], a[24:27], v[184:187], v[64:67]
buffer_load_dwordx4 v[152:155], v1, s[20:23], s90 offen offset:0 sc1
v_mfma_f32_16x16x32_bf16 v[68:71], a[28:31], v[184:187], v[68:71]
v_swap_b32 v194, v196
v_mfma_f32_16x16x32_bf16 v[72:75], a[32:35], v[184:187], v[72:75]
v_swap_b32 v195, v197
v_mfma_f32_16x16x32_bf16 v[76:79], a[36:39], v[184:187], v[76:79]
s_nop 0
v_mfma_f32_16x16x32_bf16 v[80:83], a[40:43], v[196:199], v[80:83]
buffer_load_dwordx4 v[156:159], v1, s[20:23], s90 offen offset:1024 sc1
v_mfma_f32_16x16x32_bf16 v[84:87], a[44:47], v[196:199], v[84:87]
s_add_u32 s91, s26, 46080
v_mfma_f32_16x16x32_bf16 v[88:91], a[48:51], v[196:199], v[88:91]
buffer_load_dwordx4 a[196:199], v252, s[20:23], s91 offen offset:0 sc1
v_mfma_f32_16x16x32_bf16 v[92:95], a[52:55], v[196:199], v[92:95]
v_mfma_f32_16x16x32_bf16 v[96:99], a[56:59], v[196:199], v[96:99]
buffer_load_dwordx4 v[160:163], v1, s[20:23], s90 offen offset:3072 sc1
v_mfma_f32_16x16x32_bf16 v[60:63], a[40:43], v[192:195], v[60:63]
v_mfma_f32_16x16x32_bf16 v[64:67], a[44:47], v[192:195], v[64:67]
buffer_load_dwordx4 v[164:167], v252, s[20:23], s90 offen offset:0 sc1
v_mfma_f32_16x16x32_bf16 v[68:71], a[48:51], v[192:195], v[68:71]
s_waitcnt vmcnt(20)
v_mfma_f32_16x16x32_bf16 v[72:75], a[52:55], v[192:195], v[72:75]
v_mfma_f32_16x16x32_bf16 v[76:79], a[56:59], v[192:195], v[76:79]
buffer_load_dwordx2 v[244:245], v250, s[20:23], s81 offen sc1
s_add_u32 s91, s26, 51200
v_mfma_f32_16x16x32_bf16 v[80:83], a[60:63], v[204:207], v[80:83]
buffer_load_dwordx4 a[216:219], v252, s[20:23], s91 offen offset:0 sc1
v_mfma_f32_16x16x32_bf16 v[84:87], a[64:67], v[204:207], v[84:87]
v_mfma_f32_16x16x32_bf16 v[88:91], a[68:71], v[204:207], v[88:91]
v_mfma_f32_16x16x32_bf16 v[92:95], a[72:75], v[204:207], v[92:95]
buffer_load_dwordx4 v[168:171], v1, s[20:23], s90 offen offset:2048 sc1
v_mfma_f32_16x16x32_bf16 v[96:99], a[76:79], v[204:207], v[96:99]
v_mfma_f32_16x16x32_bf16 v[60:63], a[60:63], v[200:203], v[60:63]
v_mfma_f32_16x16x32_bf16 v[64:67], a[64:67], v[200:203], v[64:67]
buffer_load_dwordx4 v[172:175], v252, s[20:23], s90 offen offset:1024 sc1
v_mfma_f32_16x16x32_bf16 v[68:71], a[68:71], v[200:203], v[68:71]
s_waitcnt vmcnt(22)
v_mfma_f32_16x16x32_bf16 v[72:75], a[72:75], v[200:203], v[72:75]
v_mfma_f32_16x16x32_bf16 v[76:79], a[76:79], v[200:203], v[76:79]
s_add_u32 s91, s26, 56320
v_mfma_f32_16x16x32_bf16 v[80:83], a[80:83], v[212:215], v[80:83]
buffer_load_dwordx4 a[236:239], v252, s[20:23], s91 offen offset:0 sc1
v_mfma_f32_16x16x32_bf16 v[84:87], a[84:87], v[212:215], v[84:87]
v_mfma_f32_16x16x32_bf16 v[88:91], a[88:91], v[212:215], v[88:91]
v_mfma_f32_16x16x32_bf16 v[92:95], a[92:95], v[212:215], v[92:95]
s_waitcnt vmcnt(22)
v_mfma_f32_16x16x32_bf16 v[96:99], a[96:99], v[212:215], v[96:99]
v_mfma_f32_16x16x32_bf16 v[60:63], a[80:83], v[208:211], v[60:63]
v_mfma_f32_16x16x32_bf16 v[64:67], a[84:87], v[208:211], v[64:67]
v_mfma_f32_16x16x32_bf16 v[68:71], a[88:91], v[208:211], v[68:71]
s_waitcnt vmcnt(20)
v_swap_b32 v218, v220
v_mfma_f32_16x16x32_bf16 v[72:75], a[92:95], v[208:211], v[72:75]
v_swap_b32 v219, v221
v_mfma_f32_16x16x32_bf16 v[76:79], a[96:99], v[208:211], v[76:79]
s_nop 0
v_mfma_f32_16x16x32_bf16 v[80:83], a[100:103], v[220:223], v[80:83]
v_mfma_f32_16x16x32_bf16 v[84:87], a[104:107], v[220:223], v[84:87]
v_mfma_f32_16x16x32_bf16 v[88:91], a[108:111], v[220:223], v[88:91]
v_mfma_f32_16x16x32_bf16 v[92:95], a[112:115], v[220:223], v[92:95]
s_waitcnt vmcnt(19)
v_mfma_f32_16x16x32_bf16 v[96:99], a[116:119], v[220:223], v[96:99]
v_mfma_f32_16x16x32_bf16 v[60:63], a[100:103], v[216:219], v[60:63]
v_mfma_f32_16x16x32_bf16 v[64:67], a[104:107], v[216:219], v[64:67]
v_mfma_f32_16x16x32_bf16 v[68:71], a[108:111], v[216:219], v[68:71]
s_waitcnt vmcnt(17)
v_mfma_f32_16x16x32_bf16 v[72:75], a[112:115], v[216:219], v[72:75]
v_mfma_f32_16x16x32_bf16 v[76:79], a[116:119], v[216:219], v[76:79]
v_mfma_f32_16x16x32_bf16 v[80:83], a[120:123], v[228:231], v[80:83]
v_mfma_f32_16x16x32_bf16 v[84:87], a[124:127], v[228:231], v[84:87]
v_mfma_f32_16x16x32_bf16 v[88:91], a[128:131], v[228:231], v[88:91]
v_mfma_f32_16x16x32_bf16 v[92:95], a[132:135], v[228:231], v[92:95]
s_waitcnt vmcnt(16)
v_mfma_f32_16x16x32_bf16 v[96:99], a[136:139], v[228:231], v[96:99]
v_mfma_f32_16x16x32_bf16 v[60:63], a[120:123], v[224:227], v[60:63]
v_mfma_f32_16x16x32_bf16 v[64:67], a[124:127], v[224:227], v[64:67]
v_mfma_f32_16x16x32_bf16 v[68:71], a[128:131], v[224:227], v[68:71]
s_waitcnt vmcnt(14)
v_mfma_f32_16x16x32_bf16 v[72:75], a[132:135], v[224:227], v[72:75]
v_mfma_f32_16x16x32_bf16 v[76:79], a[136:139], v[224:227], v[76:79]
s_waitcnt vmcnt(4)
v_cmp_ne_u64_e32 vcc, s[70:71], v[244:245]
s_mov_b32 s8, 0x200000
s_cmp_eq_u64 vcc, 0
s_cbranch_scc1 Lfb13_%=
Lfs13_%=:
buffer_load_dwordx2 v[244:245], v250, s[20:23], s81 offen sc1
s_waitcnt vmcnt(0)
v_cmp_ne_u64_e32 vcc, s[70:71], v[244:245]
s_cmp_eq_u64 vcc, 0
s_cbranch_scc1 Lfb13_%=
s_sub_u32 s8, s8, 1
s_cbranch_scc0 Lfs13_%=
Lfb13_%=:
s_add_u32 s90, s85, 98304
v_mfma_f32_16x16x32_bf16 v[80:83], a[140:143], v[140:143], v[80:83]
buffer_load_dwordx4 v[176:179], v1, s[20:23], s90 offen offset:0 sc1
v_mfma_f32_16x16x32_bf16 v[84:87], a[144:147], v[140:143], v[84:87]
v_mfma_f32_16x16x32_bf16 v[88:91], a[148:151], v[140:143], v[88:91]
v_mfma_f32_16x16x32_bf16 v[92:95], a[152:155], v[140:143], v[92:95]
buffer_load_dwordx4 v[180:183], v1, s[20:23], s90 offen offset:1024 sc1
v_mfma_f32_16x16x32_bf16 v[96:99], a[156:159], v[140:143], v[96:99]
v_mfma_f32_16x16x32_bf16 v[60:63], a[140:143], v[232:235], v[60:63]
v_mfma_f32_16x16x32_bf16 v[64:67], a[144:147], v[232:235], v[64:67]
buffer_load_dwordx4 v[184:187], v1, s[20:23], s90 offen offset:3072 sc1
v_mfma_f32_16x16x32_bf16 v[68:71], a[148:151], v[232:235], v[68:71]
v_swap_b32 v146, v148
v_mfma_f32_16x16x32_bf16 v[72:75], a[152:155], v[232:235], v[72:75]
v_swap_b32 v147, v149
v_mfma_f32_16x16x32_bf16 v[76:79], a[156:159], v[232:235], v[76:79]
s_nop 0
v_mfma_f32_16x16x32_bf16 v[80:83], a[160:163], v[148:151], v[80:83]
s_waitcnt vmcnt(4)
v_swap_b32 v170, v172
v_swap_b32 v171, v173
v_mfma_f32_16x16x32_bf16 v[80:83], a[180:183], v[156:159], v[80:83]
v_mfma_f32_16x16x32_bf16 v[80:83], a[200:203], v[164:167], v[80:83]
buffer_load_dwordx4 v[188:191], v252, s[20:23], s90 offen offset:0 sc1
s_waitcnt lgkmcnt(6)
v_mfma_f32_16x16x32_bf16 v[80:83], a[220:223], v[172:175], v[80:83]
v_mfma_f32_16x16x32_bf16 v[84:87], a[164:167], v[148:151], v[84:87]
buffer_load_dwordx4 v[192:195], v1, s[20:23], s90 offen offset:2048 sc1
v_mfma_f32_16x16x32_bf16 v[84:87], a[184:187], v[156:159], v[84:87]
v_mfma_f32_16x16x32_bf16 v[84:87], a[204:207], v[164:167], v[84:87]
buffer_load_dwordx4 v[196:199], v252, s[20:23], s90 offen offset:1024 sc1
s_waitcnt lgkmcnt(4)
v_mfma_f32_16x16x32_bf16 v[84:87], a[224:227], v[172:175], v[84:87]
s_add_u32 s90, s85, 104448
v_mfma_f32_16x16x32_bf16 v[88:91], a[168:171], v[148:151], v[88:91]
)A"
R"A(buffer_load_dwordx4 v[200:203], v1, s[20:23], s90 offen offset:0 sc1
v_mfma_f32_16x16x32_bf16 v[88:91], a[188:191], v[156:159], v[88:91]
v_mfma_f32_16x16x32_bf16 v[88:91], a[208:211], v[164:167], v[88:91]
buffer_load_dwordx4 v[204:207], v1, s[20:23], s90 offen offset:1024 sc1
s_waitcnt lgkmcnt(2)
v_mfma_f32_16x16x32_bf16 v[88:91], a[228:231], v[172:175], v[88:91]
v_mfma_f32_16x16x32_bf16 v[92:95], a[172:175], v[148:151], v[92:95]
ds_write_b128 v20, v[80:83] offset:2048
v_mfma_f32_16x16x32_bf16 v[92:95], a[192:195], v[156:159], v[92:95]
v_mfma_f32_16x16x32_bf16 v[92:95], a[212:215], v[164:167], v[92:95]
ds_write_b128 v20, v[84:87] offset:3072
s_waitcnt lgkmcnt(2)
v_mfma_f32_16x16x32_bf16 v[92:95], a[232:235], v[172:175], v[92:95]
v_mfma_f32_16x16x32_bf16 v[96:99], a[176:179], v[148:151], v[96:99]
buffer_load_dwordx4 v[208:211], v1, s[20:23], s90 offen offset:3072 sc1
v_mfma_f32_16x16x32_bf16 v[96:99], a[196:199], v[156:159], v[96:99]
v_mfma_f32_16x16x32_bf16 v[96:99], a[216:219], v[164:167], v[96:99]
buffer_load_dwordx4 v[212:215], v252, s[20:23], s90 offen offset:0 sc1
s_waitcnt vmcnt(10)
v_mfma_f32_16x16x32_bf16 v[96:99], a[236:239], v[172:175], v[96:99]
v_mfma_f32_16x16x32_bf16 v[76:79], a[176:179], v[144:147], v[76:79]
ds_write_b128 v20, v[88:91] offset:4096
v_mfma_f32_16x16x32_bf16 v[76:79], a[196:199], v[152:155], v[76:79]
v_mfma_f32_16x16x32_bf16 v[76:79], a[216:219], v[160:163], v[76:79]
ds_write_b128 v20, v[92:95] offset:5120
v_mfma_f32_16x16x32_bf16 v[76:79], a[236:239], v[168:171], v[76:79]
v_mfma_f32_16x16x32_bf16 v[68:71], a[168:171], v[144:147], v[68:71]
buffer_load_dwordx4 v[216:219], v1, s[20:23], s90 offen offset:2048 sc1
v_mfma_f32_16x16x32_bf16 v[68:71], a[188:191], v[152:155], v[68:71]
v_mfma_f32_16x16x32_bf16 v[68:71], a[208:211], v[160:163], v[68:71]
buffer_load_dwordx4 v[220:223], v252, s[20:23], s90 offen offset:1024 sc1
v_mfma_f32_16x16x32_bf16 v[68:71], a[228:231], v[168:171], v[68:71]
v_mfma_f32_16x16x32_bf16 v[72:75], a[172:175], v[144:147], v[72:75]
ds_write_b128 v20, v[96:99] offset:7168
v_mfma_f32_16x16x32_bf16 v[72:75], a[192:195], v[152:155], v[72:75]
v_mfma_f32_16x16x32_bf16 v[72:75], a[212:215], v[160:163], v[72:75]
ds_write_b128 v20, v[76:79] offset:6144
v_mfma_f32_16x16x32_bf16 v[72:75], a[232:235], v[168:171], v[72:75]
s_add_u32 s90, s85, 110592
v_mfma_f32_16x16x32_bf16 v[60:63], a[160:163], v[144:147], v[60:63]
buffer_load_dwordx4 v[224:227], v1, s[20:23], s90 offen offset:0 sc1
v_mfma_f32_16x16x32_bf16 v[60:63], a[180:183], v[152:155], v[60:63]
v_mfma_f32_16x16x32_bf16 v[60:63], a[200:203], v[160:163], v[60:63]
buffer_load_dwordx4 v[228:231], v1, s[20:23], s90 offen offset:1024 sc1
v_mfma_f32_16x16x32_bf16 v[60:63], a[220:223], v[168:171], v[60:63]
v_mfma_f32_16x16x32_bf16 v[64:67], a[164:167], v[144:147], v[64:67]
ds_write_b128 v20, v[68:71] offset:0
v_mfma_f32_16x16x32_bf16 v[64:67], a[184:187], v[152:155], v[64:67]
v_mfma_f32_16x16x32_bf16 v[64:67], a[204:207], v[160:163], v[64:67]
ds_write_b128 v20, v[72:75] offset:1024
v_mfma_f32_16x16x32_bf16 v[64:67], a[224:227], v[168:171], v[64:67]
s_waitcnt vmcnt(12)
v_mfma_f32_16x16x32_bf16 v[120:123], a[0:3], v[180:183], 0
buffer_load_dwordx4 v[232:235], v1, s[20:23], s90 offen offset:3072 sc1
s_waitcnt vmcnt(11)
v_mfma_f32_16x16x32_bf16 v[120:123], a[20:23], v[188:191], v[120:123]
v_mfma_f32_16x16x32_bf16 v[124:127], a[4:7], v[180:183], 0
v_mfma_f32_16x16x32_bf16 v[124:127], a[24:27], v[188:191], v[124:127]
buffer_load_dwordx4 v[140:143], v252, s[20:23], s90 offen offset:0 sc1
v_mfma_f32_16x16x32_bf16 v[128:131], a[8:11], v[180:183], 0
v_mfma_f32_16x16x32_bf16 v[128:131], a[28:31], v[188:191], v[128:131]
v_mfma_f32_16x16x32_bf16 v[132:135], a[12:15], v[180:183], 0
buffer_load_dwordx4 v[144:147], v1, s[20:23], s90 offen offset:2048 sc1
v_mfma_f32_16x16x32_bf16 v[132:135], a[32:35], v[188:191], v[132:135]
s_waitcnt lgkmcnt(0)
s_barrier
v_mfma_f32_16x16x32_bf16 v[136:139], a[16:19], v[180:183], 0
v_mfma_f32_16x16x32_bf16 v[136:139], a[36:39], v[188:191], v[136:139]
v_mfma_f32_16x16x32_bf16 v[100:103], a[0:3], v[176:179], v[100:103]
buffer_load_dwordx4 v[148:151], v252, s[20:23], s90 offen offset:1024 sc1
v_mfma_f32_16x16x32_bf16 v[100:103], a[20:23], v[184:187], v[100:103]
ds_read_b128 v[24:27], v21 offset:0
v_mfma_f32_16x16x32_bf16 v[104:107], a[4:7], v[176:179], v[104:107]
ds_read_b128 v[36:39], v22 offset:2048
s_add_u32 s90, s85, 116736
v_mfma_f32_16x16x32_bf16 v[104:107], a[24:27], v[184:187], v[104:107]
buffer_load_dwordx4 v[152:155], v1, s[20:23], s90 offen offset:0 sc1
v_mfma_f32_16x16x32_bf16 v[108:111], a[8:11], v[176:179], 0
ds_read_b128 v[48:51], v23 offset:4096
v_mfma_f32_16x16x32_bf16 v[108:111], a[28:31], v[184:187], v[108:111]
ds_read_b128 v[28:31], v21 offset:1024
v_mfma_f32_16x16x32_bf16 v[112:115], a[12:15], v[176:179], 0
buffer_load_dwordx4 v[156:159], v1, s[20:23], s90 offen offset:1024 sc1
v_mfma_f32_16x16x32_bf16 v[112:115], a[32:35], v[184:187], v[112:115]
s_waitcnt vmcnt(12)
v_swap_b32 v194, v196
ds_read_b128 v[40:43], v22 offset:3072
ds_read_b128 v[52:55], v23 offset:5120
v_mfma_f32_16x16x32_bf16 v[116:119], a[16:19], v[176:179], v[116:119]
v_swap_b32 v195, v197
v_mfma_f32_16x16x32_bf16 v[116:119], a[36:39], v[184:187], v[116:119]
ds_read_b128 v[32:35], v21 offset:6144
ds_read_b128 v[44:47], v22 offset:7168
ds_read_b128 v[56:59], v23 offset:7168
v_mfma_f32_16x16x32_bf16 v[120:123], a[40:43], v[196:199], v[120:123]
buffer_load_dwordx4 v[160:163], v1, s[20:23], s90 offen offset:3072 sc1
v_mfma_f32_16x16x32_bf16 v[120:123], a[60:63], v[204:207], v[120:123]
s_waitcnt lgkmcnt(8)
v_add_f32_e32 v60, v60, v24
v_add_f32_e32 v61, v61, v25
v_mfma_f32_16x16x32_bf16 v[124:127], a[44:47], v[196:199], v[124:127]
v_add_f32_e32 v62, v62, v26
v_add_f32_e32 v63, v63, v27
v_mfma_f32_16x16x32_bf16 v[124:127], a[64:67], v[204:207], v[124:127]
buffer_load_dwordx4 v[164:167], v252, s[20:23], s90 offen offset:0 sc1
v_mfma_f32_16x16x32_bf16 v[128:131], a[48:51], v[196:199], v[128:131]
s_waitcnt lgkmcnt(7)
v_add_f32_e32 v60, v60, v36
v_add_f32_e32 v61, v61, v37
v_mfma_f32_16x16x32_bf16 v[128:131], a[68:71], v[204:207], v[128:131]
v_add_f32_e32 v62, v62, v38
v_add_f32_e32 v63, v63, v39
v_mfma_f32_16x16x32_bf16 v[132:135], a[52:55], v[196:199], v[132:135]
buffer_load_dwordx4 v[168:171], v1, s[20:23], s90 offen offset:2048 sc1
v_mfma_f32_16x16x32_bf16 v[132:135], a[72:75], v[204:207], v[132:135]
s_waitcnt lgkmcnt(6)
v_add_f32_e32 v60, v60, v48
v_add_f32_e32 v61, v61, v49
v_mfma_f32_16x16x32_bf16 v[136:139], a[56:59], v[196:199], v[136:139]
v_add_f32_e32 v62, v62, v50
v_add_f32_e32 v63, v63, v51
v_mfma_f32_16x16x32_bf16 v[136:139], a[76:79], v[204:207], v[136:139]
v_cvt_pk_bf16_f32 v60, v60, v61
v_cvt_pk_bf16_f32 v61, v62, v63
v_mfma_f32_16x16x32_bf16 v[100:103], a[40:43], v[192:195], v[100:103]
buffer_load_dwordx4 v[172:175], v252, s[20:23], s90 offen offset:1024 sc1
v_mfma_f32_16x16x32_bf16 v[100:103], a[60:63], v[200:203], v[100:103]
s_waitcnt lgkmcnt(5)
v_add_f32_e32 v64, v64, v28
v_add_f32_e32 v65, v65, v29
v_mfma_f32_16x16x32_bf16 v[104:107], a[44:47], v[192:195], v[104:107]
v_add_f32_e32 v66, v66, v30
v_add_f32_e32 v67, v67, v31
v_mfma_f32_16x16x32_bf16 v[104:107], a[64:67], v[200:203], v[104:107]
s_waitcnt lgkmcnt(4)
v_add_f32_e32 v64, v64, v40
v_add_f32_e32 v65, v65, v41
v_mfma_f32_16x16x32_bf16 v[108:111], a[48:51], v[192:195], v[108:111]
v_add_f32_e32 v66, v66, v42
v_add_f32_e32 v67, v67, v43
v_mfma_f32_16x16x32_bf16 v[108:111], a[68:71], v[200:203], v[108:111]
s_waitcnt lgkmcnt(3)
v_add_f32_e32 v64, v64, v52
v_add_f32_e32 v65, v65, v53
v_mfma_f32_16x16x32_bf16 v[112:115], a[52:55], v[192:195], v[112:115]
v_add_f32_e32 v66, v66, v54
v_add_f32_e32 v67, v67, v55
v_mfma_f32_16x16x32_bf16 v[112:115], a[72:75], v[200:203], v[112:115]
s_waitcnt vmcnt(12)
v_swap_b32 v218, v220
v_cvt_pk_bf16_f32 v62, v64, v65
v_mfma_f32_16x16x32_bf16 v[116:119], a[56:59], v[192:195], v[116:119]
v_swap_b32 v219, v221
v_cvt_pk_bf16_f32 v63, v66, v67
v_mfma_f32_16x16x32_bf16 v[116:119], a[76:79], v[200:203], v[116:119]
)A"
R"A(buffer_store_dwordx4 v[60:63], v16, s[36:39], s31 offen offset:0
v_mfma_f32_16x16x32_bf16 v[120:123], a[80:83], v[212:215], v[120:123]
s_waitcnt lgkmcnt(2)
v_add_f32_e32 v76, v76, v32
v_add_f32_e32 v77, v77, v33
v_mfma_f32_16x16x32_bf16 v[120:123], a[100:103], v[220:223], v[120:123]
v_add_f32_e32 v78, v78, v34
v_add_f32_e32 v79, v79, v35
v_mfma_f32_16x16x32_bf16 v[124:127], a[84:87], v[212:215], v[124:127]
s_waitcnt lgkmcnt(1)
v_add_f32_e32 v76, v76, v44
v_add_f32_e32 v77, v77, v45
v_mfma_f32_16x16x32_bf16 v[124:127], a[104:107], v[220:223], v[124:127]
v_add_f32_e32 v78, v78, v46
v_add_f32_e32 v79, v79, v47
v_mfma_f32_16x16x32_bf16 v[128:131], a[88:91], v[212:215], v[128:131]
s_waitcnt lgkmcnt(0)
v_add_f32_e32 v76, v76, v56
v_add_f32_e32 v77, v77, v57
v_mfma_f32_16x16x32_bf16 v[128:131], a[108:111], v[220:223], v[128:131]
v_add_f32_e32 v78, v78, v58
v_add_f32_e32 v79, v79, v59
v_mfma_f32_16x16x32_bf16 v[132:135], a[92:95], v[212:215], v[132:135]
v_cvt_pk_bf16_f32 v76, v76, v77
v_cvt_pk_bf16_f32 v77, v78, v79
v_mfma_f32_16x16x32_bf16 v[132:135], a[112:115], v[220:223], v[132:135]
buffer_store_dwordx2 v[76:77], v17, s[36:39], s31 offen
v_mfma_f32_16x16x32_bf16 v[136:139], a[96:99], v[212:215], v[136:139]
s_add_u32 s31, s31, 40960
v_mfma_f32_16x16x32_bf16 v[136:139], a[116:119], v[220:223], v[136:139]
v_mfma_f32_16x16x32_bf16 v[100:103], a[80:83], v[208:211], v[100:103]
v_mfma_f32_16x16x32_bf16 v[100:103], a[100:103], v[216:219], v[100:103]
v_mfma_f32_16x16x32_bf16 v[104:107], a[84:87], v[208:211], v[104:107]
v_mfma_f32_16x16x32_bf16 v[104:107], a[104:107], v[216:219], v[104:107]
v_mfma_f32_16x16x32_bf16 v[108:111], a[88:91], v[208:211], v[108:111]
v_mfma_f32_16x16x32_bf16 v[108:111], a[108:111], v[216:219], v[108:111]
v_mfma_f32_16x16x32_bf16 v[112:115], a[92:95], v[208:211], v[112:115]
v_mfma_f32_16x16x32_bf16 v[112:115], a[112:115], v[216:219], v[112:115]
s_waitcnt vmcnt(10)
v_mfma_f32_16x16x32_bf16 v[116:119], a[96:99], v[208:211], v[116:119]
v_mfma_f32_16x16x32_bf16 v[116:119], a[116:119], v[216:219], v[116:119]
v_mfma_f32_16x16x32_bf16 v[120:123], a[120:123], v[228:231], v[120:123]
v_mfma_f32_16x16x32_bf16 v[120:123], a[140:143], v[140:143], v[120:123]
v_mfma_f32_16x16x32_bf16 v[124:127], a[124:127], v[228:231], v[124:127]
v_mfma_f32_16x16x32_bf16 v[124:127], a[144:147], v[140:143], v[124:127]
v_mfma_f32_16x16x32_bf16 v[128:131], a[128:131], v[228:231], v[128:131]
v_mfma_f32_16x16x32_bf16 v[128:131], a[148:151], v[140:143], v[128:131]
v_mfma_f32_16x16x32_bf16 v[132:135], a[132:135], v[228:231], v[132:135]
v_mfma_f32_16x16x32_bf16 v[132:135], a[152:155], v[140:143], v[132:135]
v_mfma_f32_16x16x32_bf16 v[136:139], a[136:139], v[228:231], v[136:139]
v_mfma_f32_16x16x32_bf16 v[136:139], a[156:159], v[140:143], v[136:139]
v_mfma_f32_16x16x32_bf16 v[100:103], a[120:123], v[224:227], v[100:103]
v_mfma_f32_16x16x32_bf16 v[100:103], a[140:143], v[232:235], v[100:103]
v_mfma_f32_16x16x32_bf16 v[104:107], a[124:127], v[224:227], v[104:107]
v_mfma_f32_16x16x32_bf16 v[104:107], a[144:147], v[232:235], v[104:107]
v_mfma_f32_16x16x32_bf16 v[108:111], a[128:131], v[224:227], v[108:111]
v_mfma_f32_16x16x32_bf16 v[108:111], a[148:151], v[232:235], v[108:111]
v_mfma_f32_16x16x32_bf16 v[112:115], a[132:135], v[224:227], v[112:115]
v_mfma_f32_16x16x32_bf16 v[112:115], a[152:155], v[232:235], v[112:115]
s_waitcnt vmcnt(8)
v_swap_b32 v146, v148
v_mfma_f32_16x16x32_bf16 v[116:119], a[136:139], v[224:227], v[116:119]
v_swap_b32 v147, v149
v_mfma_f32_16x16x32_bf16 v[116:119], a[156:159], v[232:235], v[116:119]
s_nop 0
v_mfma_f32_16x16x32_bf16 v[120:123], a[160:163], v[148:151], v[120:123]
s_waitcnt vmcnt(2)
v_swap_b32 v170, v172
v_swap_b32 v171, v173
v_mfma_f32_16x16x32_bf16 v[120:123], a[180:183], v[156:159], v[120:123]
v_mfma_f32_16x16x32_bf16 v[120:123], a[200:203], v[164:167], v[120:123]
v_mfma_f32_16x16x32_bf16 v[120:123], a[220:223], v[172:175], v[120:123]
v_mfma_f32_16x16x32_bf16 v[124:127], a[164:167], v[148:151], v[124:127]
v_mfma_f32_16x16x32_bf16 v[124:127], a[184:187], v[156:159], v[124:127]
v_mfma_f32_16x16x32_bf16 v[124:127], a[204:207], v[164:167], v[124:127]
v_mfma_f32_16x16x32_bf16 v[124:127], a[224:227], v[172:175], v[124:127]
v_mfma_f32_16x16x32_bf16 v[128:131], a[168:171], v[148:151], v[128:131]
v_mfma_f32_16x16x32_bf16 v[128:131], a[188:191], v[156:159], v[128:131]
v_mfma_f32_16x16x32_bf16 v[128:131], a[208:211], v[164:167], v[128:131]
v_mfma_f32_16x16x32_bf16 v[128:131], a[228:231], v[172:175], v[128:131]
v_mfma_f32_16x16x32_bf16 v[132:135], a[172:175], v[148:151], v[132:135]
ds_write_b128 v20, v[120:123] offset:34816
v_mfma_f32_16x16x32_bf16 v[132:135], a[192:195], v[156:159], v[132:135]
v_mfma_f32_16x16x32_bf16 v[132:135], a[212:215], v[164:167], v[132:135]
ds_write_b128 v20, v[124:127] offset:35840
v_mfma_f32_16x16x32_bf16 v[132:135], a[232:235], v[172:175], v[132:135]
v_mfma_f32_16x16x32_bf16 v[136:139], a[176:179], v[148:151], v[136:139]
v_mfma_f32_16x16x32_bf16 v[136:139], a[196:199], v[156:159], v[136:139]
v_mfma_f32_16x16x32_bf16 v[136:139], a[216:219], v[164:167], v[136:139]
v_mfma_f32_16x16x32_bf16 v[136:139], a[236:239], v[172:175], v[136:139]
v_mfma_f32_16x16x32_bf16 v[116:119], a[176:179], v[144:147], v[116:119]
ds_write_b128 v20, v[128:131] offset:36864
v_mfma_f32_16x16x32_bf16 v[116:119], a[196:199], v[152:155], v[116:119]
v_mfma_f32_16x16x32_bf16 v[116:119], a[216:219], v[160:163], v[116:119]
ds_write_b128 v20, v[132:135] offset:37888
v_mfma_f32_16x16x32_bf16 v[116:119], a[236:239], v[168:171], v[116:119]
v_mfma_f32_16x16x32_bf16 v[108:111], a[168:171], v[144:147], v[108:111]
v_mfma_f32_16x16x32_bf16 v[108:111], a[188:191], v[152:155], v[108:111]
v_mfma_f32_16x16x32_bf16 v[108:111], a[208:211], v[160:163], v[108:111]
v_mfma_f32_16x16x32_bf16 v[108:111], a[228:231], v[168:171], v[108:111]
v_mfma_f32_16x16x32_bf16 v[112:115], a[172:175], v[144:147], v[112:115]
ds_write_b128 v20, v[136:139] offset:39936
v_mfma_f32_16x16x32_bf16 v[112:115], a[192:195], v[152:155], v[112:115]
v_mfma_f32_16x16x32_bf16 v[112:115], a[212:215], v[160:163], v[112:115]
ds_write_b128 v20, v[116:119] offset:38912
v_mfma_f32_16x16x32_bf16 v[112:115], a[232:235], v[168:171], v[112:115]
v_mfma_f32_16x16x32_bf16 v[100:103], a[160:163], v[144:147], v[100:103]
v_mfma_f32_16x16x32_bf16 v[100:103], a[180:183], v[152:155], v[100:103]
v_mfma_f32_16x16x32_bf16 v[100:103], a[200:203], v[160:163], v[100:103]
v_mfma_f32_16x16x32_bf16 v[100:103], a[220:223], v[168:171], v[100:103]
v_mfma_f32_16x16x32_bf16 v[104:107], a[164:167], v[144:147], v[104:107]
ds_write_b128 v20, v[108:111] offset:32768
v_mfma_f32_16x16x32_bf16 v[104:107], a[184:187], v[152:155], v[104:107]
v_mfma_f32_16x16x32_bf16 v[104:107], a[204:207], v[160:163], v[104:107]
ds_write_b128 v20, v[112:115] offset:33792
v_mfma_f32_16x16x32_bf16 v[104:107], a[224:227], v[168:171], v[104:107]
s_waitcnt lgkmcnt(0)
s_barrier
ds_read_b128 v[24:27], v21 offset:32768
ds_read_b128 v[36:39], v22 offset:34816
ds_read_b128 v[48:51], v23 offset:36864
ds_read_b128 v[28:31], v21 offset:33792
ds_read_b128 v[40:43], v22 offset:35840
ds_read_b128 v[52:55], v23 offset:37888
ds_read_b128 v[32:35], v21 offset:38912
ds_read_b128 v[44:47], v22 offset:39936
ds_read_b128 v[56:59], v23 offset:39936
s_waitcnt lgkmcnt(8)
v_pk_add_f32 v[100:101], v[100:101], v[24:25]
v_pk_add_f32 v[102:103], v[102:103], v[26:27]
s_waitcnt lgkmcnt(7)
v_pk_add_f32 v[100:101], v[100:101], v[36:37]
v_pk_add_f32 v[102:103], v[102:103], v[38:39]
s_waitcnt lgkmcnt(6)
v_pk_add_f32 v[100:101], v[100:101], v[48:49]
v_pk_add_f32 v[102:103], v[102:103], v[50:51]
v_cvt_pk_bf16_f32 v100, v100, v101
v_cvt_pk_bf16_f32 v101, v102, v103
s_waitcnt lgkmcnt(5)
v_pk_add_f32 v[104:105], v[104:105], v[28:29]
v_pk_add_f32 v[106:107], v[106:107], v[30:31]
s_waitcnt lgkmcnt(4)
v_pk_add_f32 v[104:105], v[104:105], v[40:41]
v_pk_add_f32 v[106:107], v[106:107], v[42:43]
s_waitcnt lgkmcnt(3)
v_pk_add_f32 v[104:105], v[104:105], v[52:53]
v_pk_add_f32 v[106:107], v[106:107], v[54:55]
v_cvt_pk_bf16_f32 v102, v104, v105
)A"
R"A(v_cvt_pk_bf16_f32 v103, v106, v107
buffer_store_dwordx4 v[100:103], v16, s[36:39], s31 offen offset:0
s_waitcnt lgkmcnt(2)
v_pk_add_f32 v[116:117], v[116:117], v[32:33]
v_pk_add_f32 v[118:119], v[118:119], v[34:35]
s_waitcnt lgkmcnt(1)
v_pk_add_f32 v[116:117], v[116:117], v[44:45]
v_pk_add_f32 v[118:119], v[118:119], v[46:47]
s_waitcnt lgkmcnt(0)
v_pk_add_f32 v[116:117], v[116:117], v[56:57]
v_pk_add_f32 v[118:119], v[118:119], v[58:59]
v_cvt_pk_bf16_f32 v116, v116, v117
v_cvt_pk_bf16_f32 v117, v118, v119
buffer_store_dwordx2 v[116:117], v17, s[36:39], s31 offen
s_endpgm
Lend_%=:
)A"
      :
      : "s"(__builtin_amdgcn_kernarg_segment_ptr()), "s"(blockIdx.x), "s"(w), "s"(lds0)
      : "v0", "v1", "v2", "v3", "v4", "v5", "v6", "v7", "v8", "v9", "v10", "v11", "v12", "v13", "v14", "v15", "v16", "v17", "v18", "v19", "v20", "v21", "v22", "v23", "v24", "v25", "v26", "v27", "v28", "v29", "v30", "v31", "v32", "v33", "v34", "v35", "v36", "v37", "v38", "v39", "v40", "v41", "v42", "v43", "v44", "v45", "v46", "v47", "v48", "v49", "v50", "v51", "v52", "v53", "v54", "v55", "v56", "v57", "v58", "v59", "v60", "v61", "v62", "v63", "v64", "v65", "v66", "v67", "v68", "v69", "v70", "v71", "v72", "v73", "v74", "v75", "v76", "v77", "v78", "v79", "v80", "v81", "v82", "v83", "v84", "v85", "v86", "v87", "v88", "v89", "v90", "v91", "v92", "v93", "v94", "v95", "v96", "v97", "v98", "v99", "v100", "v101", "v102", "v103", "v104", "v105", "v106", "v107", "v108", "v109", "v110", "v111", "v112", "v113", "v114", "v115", "v116", "v117", "v118", "v119", "v120", "v121", "v122", "v123", "v124", "v125", "v126", "v127", "v128", "v129", "v130", "v131", "v132", "v133", "v134", "v135", "v136", "v137", "v138", "v139", "v140", "v141", "v142", "v143", "v144", "v145", "v146", "v147", "v148", "v149", "v150", "v151", "v152", "v153", "v154", "v155", "v156", "v157", "v158", "v159", "v160", "v161", "v162", "v163", "v164", "v165", "v166", "v167", "v168", "v169", "v170", "v171", "v172", "v173", "v174", "v175", "v176", "v177", "v178", "v179", "v180", "v181", "v182", "v183", "v184", "v185", "v186", "v187", "v188", "v189", "v190", "v191", "v192", "v193", "v194", "v195", "v196", "v197", "v198", "v199", "v200", "v201", "v202", "v203", "v204", "v205", "v206", "v207", "v208", "v209", "v210", "v211", "v212", "v213", "v214", "v215", "v216", "v217", "v218", "v219", "v220", "v221", "v222", "v223", "v224", "v225", "v226", "v227", "v228", "v229", "v230", "v231", "v232", "v233", "v234", "v235", "v236", "v237", "v238", "v239", "v240", "v241", "v242", "v243", "v244", "v245", "v246", "v247", "v248", "v249", "v250", "v251", "v252", "v253", "v254", "v255", "a0", "a1", "a2", "a3", "a4", "a5", "a6", "a7", "a8", "a9", "a10", "a11", "a12", "a13", "a14", "a15", "a16", "a17", "a18", "a19", "a20", "a21", "a22", "a23", "a24", "a25", "a26", "a27", "a28", "a29", "a30", "a31", "a32", "a33", "a34", "a35", "a36", "a37", "a38", "a39", "a40", "a41", "a42", "a43", "a44", "a45", "a46", "a47", "a48", "a49", "a50", "a51", "a52", "a53", "a54", "a55", "a56", "a57", "a58", "a59", "a60", "a61", "a62", "a63", "a64", "a65", "a66", "a67", "a68", "a69", "a70", "a71", "a72", "a73", "a74", "a75", "a76", "a77", "a78", "a79", "a80", "a81", "a82", "a83", "a84", "a85", "a86", "a87", "a88", "a89", "a90", "a91", "a92", "a93", "a94", "a95", "a96", "a97", "a98", "a99", "a100", "a101", "a102", "a103", "a104", "a105", "a106", "a107", "a108", "a109", "a110", "a111", "a112", "a113", "a114", "a115", "a116", "a117", "a118", "a119", "a120", "a121", "a122", "a123", "a124", "a125", "a126", "a127", "a128", "a129", "a130", "a131", "a132", "a133", "a134", "a135", "a136", "a137", "a138", "a139", "a140", "a141", "a142", "a143", "a144", "a145", "a146", "a147", "a148", "a149", "a150", "a151", "a152", "a153", "a154", "a155", "a156", "a157", "a158", "a159", "a160", "a161", "a162", "a163", "a164", "a165", "a166", "a167", "a168", "a169", "a170", "a171", "a172", "a173", "a174", "a175", "a176", "a177", "a178", "a179", "a180", "a181", "a182", "a183", "a184", "a185", "a186", "a187", "a188", "a189", "a190", "a191", "a192", "a193", "a194", "a195", "a196", "a197", "a198", "a199", "a200", "a201", "a202", "a203", "a204", "a205", "a206", "a207", "a208", "a209", "a210", "a211", "a212", "a213", "a214", "a215", "a216", "a217", "a218", "a219", "a220", "a221", "a222", "a223", "a224", "a225", "a226", "a227", "a228", "a229", "a230", "a231", "a232", "a233", "a234", "a235", "a236", "a237", "a238", "a239", "a240", "a241", "a242", "a243", "a244", "a245", "a246", "a247", "a248", "a249", "a250", "a251", "a252", "a253", "a254", "a255", "s8", "s9", "s10", "s11", "s12", "s13", "s14", "s15", "s16", "s17", "s18", "s19", "s20", "s21", "s22", "s23", "s24", "s25", "s26", "s27", "s28", "s29", "s30", "s31", "s32", "s33", "s34", "s35", "s36", "s37", "s38", "s39", "s40", "s41", "s42", "s43", "s44", "s45", "s46", "s47", "s48", "s49", "s50", "s51", "s52", "s53", "s54", "s55", "s56", "s57", "s58", "s59", "s60", "s61", "s62", "s63", "s64", "s65", "s66", "s67", "s68", "s69", "s70", "s71", "s72", "s73", "s74", "s75", "s76", "s77", "s78", "s79", "s80", "s81", "s82", "s83", "s84", "s85", "s86", "s87", "s88", "s89", "s90", "s91", "s92", "s93", "s94", "s95", "s96", "s97", "s98", "s99", "vcc", "scc", "memory");
}

extern "C" void kernel_launch(void* const* d_in, const int* in_sizes, int n_in, void* d_out, int out_size, void* d_ws, size_t ws_size,
                              hipStream_t stream) {
  Params p;
  p.x = d_in[0]; p.cr = d_in[1]; p.ci = d_in[2]; p.dr = d_in[3]; p.di = d_in[4];
  p.w1 = d_in[6]; p.b1 = d_in[7]; p.w2 = d_in[8]; p.b2 = d_in[9];
  p.out = d_out; p.ws = d_ws;
  fnet_fused<<<256, 256, 0, stream>>>(p);
}
```
